```python
import torch

def _softmax(x, axis=-1):
    return torch.softmax(x, dim=axis)
def _gelu(x):
    return torch.nn.functional.gelu(x, approximate="tanh")
def _silu(x):
    return torch.nn.functional.silu(x)
def _sigmoid(x):
    return torch.sigmoid(x)
def _layernorm(x, axis=-1, eps=1e-5):
    m = x.mean(dim=axis, keepdim=True)
    v = x.var(dim=axis, keepdim=True, unbiased=False)
    return (x - m) / torch.sqrt(v + eps)
def _scatter_add(tgt, idx, src, axis=0):
    return tgt.clone().index_add_(axis, idx, src)
def _segment_sum(src, seg, n):
    out = torch.zeros((n,) + src.shape[1:], dtype=src.dtype)
    return out.index_add_(0, seg, src)
def _pad(x, pw):
    flat = tuple(v for pair in reversed(pw) for v in pair)
    return torch.nn.functional.pad(x, flat)

kernel_name = 'concat_heads__sliding_block_cached__sliding_block_cached__decode'

def setup_inputs():

    _w = (lambda a: (lambda t: t.to(torch.bfloat16) if t.is_floating_point() else t)(torch.as_tensor(a)))
    def _A_setup():
        B, Sq, D = 2, 1, 512
        Hq, Hkv, Sk, Dh = 32, 8, 8192, 128
        assert Hq % Hkv == 0, (Hq, Hkv)
        return {
            "x": _w((lambda *s: torch.randn(*s, dtype=torch.bfloat16) * 0.8)(B, Sq, D)),
            "k_cache": _w((lambda *s: torch.randn(*s, dtype=torch.bfloat16) * 0.8)(B, Hkv, Sk, Dh)),
            "v_cache": _w((lambda *s: torch.randn(*s, dtype=torch.bfloat16) * 0.05)(B, Hkv, Sk, Dh)),
            "wq": _w((lambda *s: torch.randn(*s, dtype=torch.bfloat16) * 0.8)(D, Hq * Dh) * (D ** -0.5)),
            "wo": _w((lambda *s: torch.randn(*s, dtype=torch.bfloat16) * 0.05)(Hq * Dh, D) * ((Hq * Dh) ** -0.5)),
        }
    _A_in = _A_setup()

    def _B_setup():
        B, Sq, D = 2, 1, 512
        Hq, Hkv, Sk, Dh = 32, 8, 8192, 128
        assert Hq % Hkv == 0, (Hq, Hkv)
        return {
            "x": _w((lambda *s: torch.randn(*s, dtype=torch.bfloat16) * 0.8)(B, Sq, D)),
            "k_cache": _w((lambda *s: torch.randn(*s, dtype=torch.bfloat16) * 0.8)(B, Hkv, Sk, Dh)),
            "v_cache": _w((lambda *s: torch.randn(*s, dtype=torch.bfloat16) * 0.05)(B, Hkv, Sk, Dh)),
            "wq": _w((lambda *s: torch.randn(*s, dtype=torch.bfloat16) * 0.8)(D, Hq * Dh) * (D ** -0.5)),
            "wo": _w((lambda *s: torch.randn(*s, dtype=torch.bfloat16) * 0.05)(Hq * Dh, D) * ((Hq * Dh) ** -0.5)),
        }
    _B_in = _B_setup()

    return {
        "A_x": _A_in["x"],
        "A_k_cache": _A_in["k_cache"],
        "A_v_cache": _A_in["v_cache"],
        "A_wq": _A_in["wq"],
        "A_wo": _A_in["wo"],
        "B_x": _B_in["x"],
        "B_k_cache": _B_in["k_cache"],
        "B_v_cache": _B_in["v_cache"],
        "B_wq": _B_in["wq"],
        "B_wo": _B_in["wo"],
    }

def reference(A_x, A_k_cache, A_v_cache, A_wq, A_wo, B_x, B_k_cache, B_v_cache, B_wq, B_wo):
    def _A_ref(x, k_cache, v_cache, wq, wo):
        B, Sq, D = x.shape
        Hq, Dh = 32, 128
        Hkv, Sk = k_cache.shape[1], k_cache.shape[2]
        W = min(4096, max(256, Sk // 4))
        rep = Hq // Hkv
        q = (lambda x, *d: x.permute(*d))((x @ wq).reshape(B, Sq, Hq, Dh), 0, 2, 1, 3)
        k = (lambda x, n, axis: torch.repeat_interleave(x, n, dim=axis))(k_cache[:, :, Sk - W:, :], rep, axis=1)
        v = (lambda x, n, axis: torch.repeat_interleave(x, n, dim=axis))(v_cache[:, :, Sk - W:, :], rep, axis=1)
        scores = torch.einsum("bhqd,bhkd->bhqk", q, k) * (Dh ** -0.5)
        p = _softmax(scores, axis=-1)
        o = torch.einsum("bhqk,bhkd->bhqd", p, v)
        return (lambda x, *d: x.permute(*d))(o, 0, 2, 1, 3).reshape(B, Sq, Hq * Dh) @ wo

    def _B_ref(x, k_cache, v_cache, wq, wo):
        B, Sq, D = x.shape
        Hq, Dh = 32, 128
        Hkv, Sk = k_cache.shape[1], k_cache.shape[2]
        W = min(4096, max(256, Sk // 4))
        rep = Hq // Hkv
        q = (lambda x, *d: x.permute(*d))((x @ wq).reshape(B, Sq, Hq, Dh), 0, 2, 1, 3)
        k = (lambda x, n, axis: torch.repeat_interleave(x, n, dim=axis))(k_cache[:, :, Sk - W:, :], rep, axis=1)
        v = (lambda x, n, axis: torch.repeat_interleave(x, n, dim=axis))(v_cache[:, :, Sk - W:, :], rep, axis=1)
        scores = torch.einsum("bhqd,bhkd->bhqk", q, k) * (Dh ** -0.5)
        p = _softmax(scores, axis=-1)
        o = torch.einsum("bhqk,bhkd->bhqd", p, v)
        return (lambda x, *d: x.permute(*d))(o, 0, 2, 1, 3).reshape(B, Sq, Hq * Dh) @ wo

    a = _A_ref(A_x, A_k_cache, A_v_cache, A_wq, A_wo)
    b = _B_ref(B_x, B_k_cache, B_v_cache, B_wq, B_wo)
    return torch.concatenate([a, b], axis=-1)
```

```cpp
#include <hip/hip_runtime.h>
#include <hip/hip_bf16.h>
#include <stdint.h>

typedef unsigned short u16;
__device__ __forceinline__ float bf2f(u16 x) { return __uint_as_float(((unsigned)x) << 16); }
__device__ __forceinline__ u16 f2bf(float f) { __hip_bfloat16 h = __float2bfloat16(f); return *reinterpret_cast<u16*>(&h); }

constexpr int D = 512, HQ = 32, HKV = 8, SK = 8192, DH = 128, W = 2048, NB = 2;

struct Br { const u16* x; const u16* k; const u16* v; const u16* wq; const u16* wo; };

__global__ void k_q(Br a, Br b, float* q) {
  int br = blockIdx.y; const Br& p = br ? b : a;
  int n = blockIdx.x * 256 + threadIdx.x;
  float s0 = 0, s1 = 0;
  for (int k = 0; k < D; ++k) { float w = bf2f(p.wq[(size_t)k * 4096 + n]); s0 += bf2f(p.x[k]) * w; s1 += bf2f(p.x[D + k]) * w; }
  q[(br * 2 + 0) * 4096 + n] = bf2f(f2bf(s0));
  q[(br * 2 + 1) * 4096 + n] = bf2f(f2bf(s1));
}
__global__ void k_attn(Br a, Br b_, const float* q, float* o) {
  int h = blockIdx.x, b = blockIdx.y, br = blockIdx.z; const Br& p = br ? b_ : a;
  __shared__ float sc[W]; __shared__ float red[256]; __shared__ float qs[DH];
  int t = threadIdx.x;
  if (t < DH) qs[t] = q[(br * 2 + b) * 4096 + h * DH + t];
  __syncthreads();
  const u16* kp = p.k + ((size_t)(b * HKV + h / 4) * SK + (SK - W)) * DH;
  const u16* vp = p.v + ((size_t)(b * HKV + h / 4) * SK + (SK - W)) * DH;
  float m = -INFINITY;
  for (int j = t; j < W; j += 256) { float s = 0; for (int d = 0; d < DH; ++d) s += qs[d] * bf2f(kp[(size_t)j * DH + d]); s *= 0.08838834764831845f; sc[j] = s; m = fmaxf(m, s); }
  red[t] = m; __syncthreads();
  for (int s = 128; s > 0; s >>= 1) { if (t < s) red[t] = fmaxf(red[t], red[t + s]); __syncthreads(); }
  m = red[0]; __syncthreads();
  float l = 0;
  for (int j = t; j < W; j += 256) { float e = __expf(sc[j] - m); sc[j] = e; l += e; }
  red[t] = l; __syncthreads();
  for (int s = 128; s > 0; s >>= 1) { if (t < s) red[t] += red[t + s]; __syncthreads(); }
  l = red[0]; __syncthreads();
  int d = t & 127, half = t >> 7; float acc = 0;
  for (int j = half; j < W; j += 2) acc += sc[j] * bf2f(vp[(size_t)j * DH + d]);
  red[t] = acc; __syncthreads();
  if (t < 128) o[(br * 2 + b) * 4096 + h * DH + t] = bf2f(f2bf((red[t] + red[t + 128]) / l));
}
__global__ void k_o(Br a, Br b_, const float* o, u16* out) {
  int br = blockIdx.y; const Br& p = br ? b_ : a;
  int n = blockIdx.x * 64 + (threadIdx.x & 63); int ks = threadIdx.x >> 6;
  __shared__ float red[2][16][64];
  float s0 = 0, s1 = 0;
  for (int k = ks; k < 4096; k += 16) { float w = bf2f(p.wo[(size_t)k * D + n]); s0 += o[(br * 2 + 0) * 4096 + k] * w; s1 += o[(br * 2 + 1) * 4096 + k] * w; }
  red[0][ks][threadIdx.x & 63] = s0; red[1][ks][threadIdx.x & 63] = s1; __syncthreads();
  if (threadIdx.x < 128) { int b = threadIdx.x >> 6, c = threadIdx.x & 63; float s = 0; for (int i = 0; i < 16; ++i) s += red[b][i][c];
    out[b * 1024 + br * 512 + blockIdx.x * 64 + c] = f2bf(s); }
}

extern "C" void kernel_launch(void* const* d_in, const int* in_sizes, int n_in, void* d_out, int out_size, void* d_ws, size_t ws_size, hipStream_t stream) {
  Br a{(const u16*)d_in[0], (const u16*)d_in[1], (const u16*)d_in[2], (const u16*)d_in[3], (const u16*)d_in[4]};
  Br b{(const u16*)d_in[5], (const u16*)d_in[6], (const u16*)d_in[7], (const u16*)d_in[8], (const u16*)d_in[9]};
  float* q = (float*)d_ws; float* o = q + 4 * 4096;
  k_q<<<dim3(16, 2), 256, 0, stream>>>(a, b, q);
  k_attn<<<dim3(32, 2, 2), 256, 0, stream>>>(a, b, q, o);
  k_o<<<dim3(8, 2), 1024, 0, stream>>>(a, b, o, (u16*)d_out);
}
```

```cpp
#include <hip/hip_runtime.h>
#include <hip/hip_bf16.h>
#include <stdint.h>

typedef unsigned u32;
typedef unsigned long long u64;
typedef u32 v4u __attribute__((ext_vector_type(4)));
typedef u32 v2u __attribute__((ext_vector_type(2)));
typedef float v4f __attribute__((ext_vector_type(4)));
typedef float v2f __attribute__((ext_vector_type(2)));
typedef __bf16 v8b __attribute__((ext_vector_type(8)));
typedef __bf16 v2b __attribute__((ext_vector_type(2)));
typedef short v4s __attribute__((ext_vector_type(4)));
#define LAS __attribute__((address_space(3)))
typedef __amdgpu_buffer_rsrc_t rsrc_t;

#define STAMP(i)
#define STAMP_DECL
#define STAMP_FLUSH

constexpr int DM = 512, HQ = 32, HKV = 8, SK = 8192, DH = 128, WIN = 2048;
constexpr int AUX_NT = 2, AUX_SC1 = 16;
constexpr int WS_E = 0;
constexpr int WS_L = 4096;
constexpr int WS_Q = 16384;
constexpr int WS_O = WS_Q + 16 * 16384;
constexpr int WS_R = WS_O + 16 * 32768;
constexpr int WS_END = WS_R + 8 * 65536;
constexpr int L_CNT = 0;
constexpr int L_ML = 64;
constexpr int L_Q = 256;
constexpr int L_QP = L_Q + 1024;
constexpr int OP_W = 2 * 1088;
constexpr int L_OP = L_QP + 4096;
constexpr int L_RED = L_OP + 4 * OP_W;
constexpr int L_END = L_RED + 8192;
constexpr u32 SPIN_CAP = 1u << 20;

struct Params { const void* x[2]; const void* k[2]; const void* v[2]; const void* wq[2]; const void* wo[2]; void* out; void* ws; };

__device__ __forceinline__ rsrc_t mk(const void* p, int bytes) { return __builtin_amdgcn_make_buffer_rsrc((void*)p, (short)0, bytes, 0x00020000); }
template <int AUX> __device__ __forceinline__ v4u ld16(rsrc_t r, int voff, int soff) { return __builtin_amdgcn_raw_buffer_load_b128(r, voff, soff, AUX); }
__device__ __forceinline__ u32 pk(float lo, float hi) { v2f f = {lo, hi}; return __builtin_bit_cast(u32, __builtin_convertvector(f, v2b)); }
__device__ __forceinline__ float blo(u32 x) { return __builtin_bit_cast(float, x << 16); }
__device__ __forceinline__ float bhi(u32 x) { return __builtin_bit_cast(float, x & 0xffff0000u); }
__device__ __forceinline__ v4f mfma32(v4u a, v4u b, v4f c) { return __builtin_amdgcn_mfma_f32_16x16x32_bf16(__builtin_bit_cast(v8b, a), __builtin_bit_cast(v8b, b), c, 0, 0, 0); }
__device__ __forceinline__ v4f mfma16(v2u a, v2u b, v4f c) { return __builtin_amdgcn_mfma_f32_16x16x16bf16_1k(__builtin_bit_cast(v4s, a), __builtin_bit_cast(v4s, b), c, 0, 0, 0); }
template <int CTRL> __device__ __forceinline__ float dpp(float x) { return __builtin_bit_cast(float, __builtin_amdgcn_mov_dpp(__builtin_bit_cast(int, x), CTRL, 0xf, 0xf, true)); }
constexpr int ROR4 = 0x124, ROR8 = 0x128;
__device__ __forceinline__ float x16max(float x) { auto s = __builtin_amdgcn_permlane16_swap(__float_as_uint(x), __float_as_uint(x), false, false); return fmaxf(__uint_as_float(s[0]), __uint_as_float(s[1])); }
__device__ __forceinline__ float x32max(float x) { auto s = __builtin_amdgcn_permlane32_swap(__float_as_uint(x), __float_as_uint(x), false, false); return fmaxf(__uint_as_float(s[0]), __uint_as_float(s[1])); }
__device__ __forceinline__ float x16sum(float x) { auto s = __builtin_amdgcn_permlane16_swap(__float_as_uint(x), __float_as_uint(x), false, false); return __uint_as_float(s[0]) + __uint_as_float(s[1]); }
__device__ __forceinline__ float x32sum(float x) { auto s = __builtin_amdgcn_permlane32_swap(__float_as_uint(x), __float_as_uint(x), false, false); return __uint_as_float(s[0]) + __uint_as_float(s[1]); }
#define CBAR() asm volatile("" ::: "memory")
#define LGKM0() asm volatile("s_waitcnt lgkmcnt(0)" ::: "memory")
#define BARRIER() do { LGKM0(); __builtin_amdgcn_s_barrier(); CBAR(); } while (0)
#define SB() __builtin_amdgcn_sched_barrier(0)
#define WG_RLX __ATOMIC_RELAXED, __HIP_MEMORY_SCOPE_WORKGROUP

__global__ void __launch_bounds__(512, 2) fused_decode(Params P) {
  __shared__ __attribute__((aligned(16))) char smem[L_END];
  LAS char* lds = (LAS char*)smem;
  STAMP_DECL
  STAMP(0);
  const int blk = blockIdx.x;
  const int G = (blk & 7) * 2 + (blk >> 7), c16 = (blk >> 3) & 15;
  const int br = G & 1, g = G >> 1;
  const int lane = threadIdx.x & 63, wave = __builtin_amdgcn_readfirstlane(threadIdx.x >> 6);
  const int c = lane >> 4, n = lane & 15;
  const int b = c16 >> 3, sp = c16 & 7;
  const int hh = c16 & 3, qt = c16 >> 2;
  const rsrc_t rws = mk(P.ws, WS_END);
  LAS u32* cnt = (LAS u32*)(lds + L_CNT);

  if (wave < 4) {
    const int w = wave;
    const rsrc_t rx = mk(P.x[br], 2 * DM * 2), rq = mk(P.wq[br], DM * HQ * DH * 2);
    const int row0 = qt * 128 + w * 32 + c * 8;
    const v4u xr = ld16<0>(rx, ((n >> 3) * DM + row0) * 2, 0);
    v4u wv[8];
    const int qoff = row0 * (HQ * DH * 2) + ((g * 4 + hh) * DH + n * 8) * 2;
#pragma unroll
    for (int t = 0; t < 8; ++t) wv[t] = ld16<AUX_NT>(rq, qoff, t * (HQ * DH * 2));
    const u32 ev = __builtin_amdgcn_raw_buffer_load_b32(rws, 0, WS_E + br * 256, AUX_SC1);
    SB();
    BARRIER();
    STAMP(1);
    __builtin_amdgcn_s_setprio(2);
    const int j = n & 7;
    const u32 hm = (j & 1) ? 0xffff0000u : 0x0000ffffu;
    u32 mk4[4];
#pragma unroll
    for (int d = 0; d < 4; ++d) mk4[d] = (j >> 1) == d ? hm : 0u;
    v4f acc = {0.f, 0.f, 0.f, 0.f};
#pragma unroll
    for (int t = 0; t < 8; ++t) {
      const u32 xx = __builtin_amdgcn_perm(xr[t >> 1], xr[t >> 1], (t & 1) ? 0x03020302u : 0x01000100u);
      const v4u a = {xx & mk4[0], xx & mk4[1], xx & mk4[2], xx & mk4[3]};
      acc = mfma32(a, wv[t], acc);
    }
    const u32 tag = __builtin_amdgcn_readfirstlane(ev) + 1u;
    *(LAS v4f*)(lds + L_QP + w * 1024 + lane * 16) = acc;
    CBAR();
    u32 arr = 0;
    if (lane == 0) arr = __hip_atomic_fetch_add(cnt + 0, 1u, WG_RLX);
    arr = __builtin_amdgcn_readfirstlane(arr);
    CBAR();
    STAMP(2);
    if (arr == 3) {
      v4f s = *(LAS v4f*)(lds + L_QP + lane * 16);
#pragma unroll
      for (int i = 1; i < 4; ++i) s += *(LAS v4f*)(lds + L_QP + i * 1024 + lane * 16);
      const v4u m = {pk(s[0], s[1]), tag, pk(s[2], s[3]), tag};
      __builtin_amdgcn_raw_buffer_store_b128(m, rws, (c >> 1) * 8192 + n * 32 + (c & 1) * 16, WS_Q + G * 16384 + (hh * 4 + qt) * 512, AUX_SC1);
    }
    {
      const int so = WS_Q + G * 16384 + b * 8192 + w * 2048;
      v4u g0, g1;
      for (u32 spin = 0;;) {
        CBAR();
        g0 = ld16<AUX_SC1>(rws, lane * 16, so);
        g1 = ld16<AUX_SC1>(rws, lane * 16 + 1024, so);
        const bool ok = g0[1] == tag && g0[3] == tag && g1[1] == tag && g1[3] == tag;
        if (__all(ok) || ++spin > SPIN_CAP) break;
        __builtin_amdgcn_s_sleep(2);
      }
      const float q0 = x32sum(blo(g0[0]) + blo(g1[0])), q1 = x32sum(bhi(g0[0]) + bhi(g1[0]));
      const float q2 = x32sum(blo(g0[2]) + blo(g1[2])), q3 = x32sum(bhi(g0[2]) + bhi(g1[2]));
      if (lane < 32) *(LAS v2u*)(lds + L_Q + w * 256 + lane * 8) = v2u{pk(q0, q1), pk(q2, q3)};
      if (lane == 0) { cnt[3] = tag; CBAR(); __hip_atomic_fetch_add(cnt + 1, 1u, WG_RLX); }
      CBAR();
    }
    STAMP(3);
    __builtin_amdgcn_s_setprio(0);
    const rsrc_t ro = mk(P.wo[br], HQ * DH * DM * 2);
    const int ooff = ((g * 4 + hh) * DH + w * 32 + c * 8) * (DM * 2) + (qt * 128 + n * 8) * 2;
#pragma unroll
    for (int t = 0; t < 8; ++t) wv[t] = ld16<AUX_NT>(ro, ooff + (t & 3) * 1024, (t >> 2) * 4096);
    SB();
    STAMP(4);
    BARRIER();
    v4u bt[8];
#pragma unroll
    for (int d = 0; d < 4; ++d)
#pragma unroll
      for (int e = 0; e < 4; ++e) {
        bt[2 * d][e] = __builtin_amdgcn_perm(wv[2 * e + 1][d], wv[2 * e][d], 0x05040100u);
        bt[2 * d + 1][e] = __builtin_amdgcn_perm(wv[2 * e + 1][d], wv[2 * e][d], 0x07060302u);
      }
#pragma unroll
    for (int t = 0; t < 8; ++t) asm volatile("" : "+v"(bt[t]));
    SB();
    STAMP(5);
    v4u o0, o1, l0, l1;
    {
      const int so = WS_O + G * 32768 + hh * 8192 + w * 128, sl = WS_L + G * 512 + hh * 128;
      for (u32 spin = 0;;) {
        CBAR();
        o0 = ld16<AUX_SC1>(rws, n * 512 + c * 32, so);
        o1 = ld16<AUX_SC1>(rws, n * 512 + c * 32 + 16, so);
        l0 = ld16<AUX_SC1>(rws, c * 32, sl);
        l1 = ld16<AUX_SC1>(rws, c * 32 + 16, sl);
        const bool ok = o0[1] == tag && o0[3] == tag && o1[1] == tag && o1[3] == tag && l0[1] == tag && l0[3] == tag && l1[1] == tag && l1[3] == tag;
        if (__all(ok) || ++spin > SPIN_CAP) break;
      }
    }
    STAMP(6);
    const v4u oa = {o0[0], o0[2], o1[0], o1[2]};
    const float a0 = __uint_as_float(l0[0]), a1 = __uint_as_float(l0[2]), a2 = __uint_as_float(l1[0]), a3 = __uint_as_float(l1[2]);
    const float mx = x16max(fmaxf(fmaxf(a0, a1), fmaxf(a2, a3)));
    float w0 = __builtin_amdgcn_exp2f(a0 - mx), w1 = __builtin_amdgcn_exp2f(a1 - mx), w2 = __builtin_amdgcn_exp2f(a2 - mx), w3 = __builtin_amdgcn_exp2f(a3 - mx);
    const float inv = __builtin_amdgcn_rcpf(x16sum((w0 + w1) + (w2 + w3)));
    w0 *= inv; w1 *= inv; w2 *= inv; w3 *= inv;
    float e[8];
#pragma unroll
    for (int jj = 0; jj < 8; ++jj) {
      const v4f d = mfma32(oa, bt[jj], v4f{0.f, 0.f, 0.f, 0.f});
      e[jj] = fmaf(d[3], w3, fmaf(d[2], w2, fmaf(d[1], w1, d[0] * w0)));
    }
    *(LAS v4f*)(lds + L_RED + w * 2048 + c * 512 + n * 16) = v4f{e[0], e[1], e[2], e[3]};
    *(LAS v4f*)(lds + L_RED + w * 2048 + c * 512 + 256 + n * 16) = v4f{e[4], e[5], e[6], e[7]};
    BARRIER();
    STAMP(7);
    if (w < 2) {
      const int idx = ((lane >> 1) & 1) * 256 + (lane >> 2) * 16 + (lane & 1) * 8;
      v2f s = {0.f, 0.f};
#pragma unroll
      for (int i = 0; i < 8; ++i) s += *(LAS v2f*)(lds + L_RED + (i >> 1) * 2048 + (2 * w + (i & 1)) * 512 + idx);
      const v4u m = {__float_as_uint(s[0]), tag, __float_as_uint(s[1]), tag};
      __builtin_amdgcn_raw_buffer_store_b128(m, rws, (lane >> 2) * 2048 + (lane & 3) * 16, WS_R + (br * 4 + qt) * 65536 + w * 32768 + (g * 4 + hh) * 64, AUX_SC1);
    }
    STAMP(8);
  } else {
    const int w = wave - 4;
    if (w == 0 && lane < 4) cnt[lane] = 0u;
    const size_t kvbase = ((size_t)(b * HKV + g) * SK + (SK - WIN) + sp * 256) * (DH * 2);
    const rsrc_t rk = mk((const char*)P.k[br] + kvbase, 65536), rv = mk((const char*)P.v[br] + kvbase, 65536);
    const int koff = w * 16384 + ((lane >> 2) & 3) * 256 + c * 64 + (lane & 3) * 16;
    const int voff = w * 16384 + lane * 16;
    __builtin_amdgcn_s_setprio(1);
    BARRIER();
    STAMP(1);
    v4u kk[16], vv[16];
#pragma unroll
    for (int t = 0; t < 16; ++t) kk[t] = ld16<AUX_NT>(rk, koff + (t & 3) * 1024, (t >> 2) * 4096);
#pragma unroll
    for (int t = 0; t < 16; ++t) vv[t] = ld16<AUX_NT>(rv, voff + (t & 3) * 1024, (t >> 2) * 4096);
    SB();
    STAMP(2);
    for (u32 spin = 0; __builtin_amdgcn_readfirstlane(__hip_atomic_load(cnt + 1, WG_RLX)) != 4u && ++spin < SPIN_CAP * 16;) __builtin_amdgcn_s_sleep(1);
    CBAR();
    STAMP(3);
    const int u = (lane >> 2) & 3, h = lane & 3;
    const v4u qf = *(LAS v4u*)(lds + L_Q + h * 256 + (4 * c + u) * 16);
    const u32 tag = __builtin_amdgcn_readfirstlane(cnt[3]);
    constexpr float CS = 0.08838834764831845f * 1.4426950408889634f;
    float s[16];
#pragma unroll
    for (int t = 0; t < 16; ++t) {
      const v4f d = mfma32(kk[t], qf, v4f{0.f, 0.f, 0.f, 0.f});
      float x = (u & 2) ? ((u & 1) ? d[3] : d[2]) : ((u & 1) ? d[1] : d[0]);
      x += dpp<ROR8>(x);
      x += dpp<ROR4>(x);
      s[t] = x * CS;
    }
    float m = s[0];
#pragma unroll
    for (int t = 1; t < 16; ++t) m = fmaxf(m, s[t]);
    m = x32max(x16max(m));
    float l = 0.f;
#pragma unroll
    for (int t = 0; t < 16; ++t) { s[t] = __builtin_amdgcn_exp2f(s[t] - m); l += s[t]; }
    l = x32sum(x16sum(l));
    if (lane < 4) *(LAS v2f*)(lds + L_ML + w * 32 + lane * 8) = v2f{m, l};
    CBAR();
    if (lane == 0) __hip_atomic_fetch_add(cnt + 2, 1u, WG_RLX);
    for (u32 spin = 0; __builtin_amdgcn_readfirstlane(__hip_atomic_load(cnt + 2, WG_RLX)) != 4u && ++spin < SPIN_CAP * 16;) {}
    CBAR();
    v2f ml[4];
#pragma unroll
    for (int i = 0; i < 4; ++i) ml[i] = *(LAS v2f*)(lds + L_ML + i * 32 + h * 8);
    const float M = fmaxf(fmaxf(ml[0][0], ml[1][0]), fmaxf(ml[2][0], ml[3][0]));
    float L = 0.f;
#pragma unroll
    for (int i = 0; i < 4; ++i) L = fmaf(__builtin_amdgcn_exp2f(ml[i][0] - M), ml[i][1], L);
    const float f = __builtin_amdgcn_exp2f(m - M) * __builtin_amdgcn_rcpf(L);
    const float lse = M + __builtin_amdgcn_logf(L);
    const u32 hm = (u & 1) ? 0xffff0000u : 0x0000ffffu;
    const u32 m0 = (u & 2) ? 0u : hm, m1 = (u & 2) ? hm : 0u;
    v2u pa[16];
#pragma unroll
    for (int t = 0; t < 16; ++t) { const float p = s[t] * f; const u32 pp = pk(p, p); pa[t] = v2u{pp & m0, pp & m1}; }
#pragma unroll
    for (int t = 0; t < 16; ++t) asm volatile("" : "+v"(pa[t]));
    SB();
    STAMP(4);
    v4f d1 = {0.f, 0.f, 0.f, 0.f}, d2 = {0.f, 0.f, 0.f, 0.f};
#pragma unroll
    for (int t = 0; t < 16; ++t) {
      d1 = mfma16(pa[t], v2u{vv[t][0], vv[t][1]}, d1);
      d2 = mfma16(pa[t], v2u{vv[t][2], vv[t][3]}, d2);
    }
    *(LAS v4f*)(lds + L_OP + w * OP_W + lane * 16) = d1;
    *(LAS v4f*)(lds + L_OP + w * OP_W + 1088 + lane * 16) = d2;
    STAMP(5);
    BARRIER();
    {
      const int a = L_OP + ((n >> 1) & 1) * 1088 + (n & 1) * 512 + (4 * w + (n >> 2)) * 16 + c * 4;
      float s0 = 0.f, s1 = 0.f;
#pragma unroll
      for (int i = 0; i < 4; ++i) { s0 += *(LAS float*)(lds + a + i * OP_W); s1 += *(LAS float*)(lds + a + i * OP_W + 256); }
      __builtin_amdgcn_raw_buffer_store_b64(v2u{pk(s0, s1), tag}, rws, c * 8192 + n * 8, WS_O + G * 32768 + c16 * 512 + w * 128, AUX_SC1);
      if (w == 0 && lane < 4) __builtin_amdgcn_raw_buffer_store_b64(v2u{__float_as_uint(lse), tag}, rws, lane * 128, WS_L + G * 512 + c16 * 8, AUX_SC1);
    }
    STAMP(6);
    BARRIER();
    STAMP(7);
    if (w == 0) {
      const int r = g * 4 + hh;
      const int so = WS_R + (br * 4 + qt) * 65536 + r * 2048;
      v4u g0, g1;
      for (u32 spin = 0;;) {
        CBAR();
        g0 = ld16<AUX_SC1>(rws, lane * 16, so);
        g1 = ld16<AUX_SC1>(rws, lane * 16 + 1024, so);
        const bool ok = g0[1] == tag && g0[3] == tag && g1[1] == tag && g1[3] == tag;
        if (__all(ok) || ++spin > SPIN_CAP) break;
      }
      float y0 = __uint_as_float(g0[0]) + __uint_as_float(g1[0]), y1 = __uint_as_float(g0[2]) + __uint_as_float(g1[2]);
      y0 += dpp<ROR8>(y0); y1 += dpp<ROR8>(y1);
      y0 += dpp<ROR4>(y0); y1 += dpp<ROR4>(y1);
      y0 = x32sum(x16sum(y0)); y1 = x32sum(x16sum(y1));
      if (lane < 4) ((u32*)P.out)[(r >> 4) * 512 + br * 256 + qt * 64 + (r & 15) * 4 + lane] = pk(y0, y1);
      if (g == 0 && c16 == 0 && lane == 0) __builtin_amdgcn_raw_buffer_store_b32(tag, rws, 0, WS_E + br * 256, AUX_SC1);
    }
    STAMP(8);
  }
  STAMP_FLUSH
}

extern "C" void kernel_launch(void* const* d_in, const int* in_sizes, int n_in, void* d_out, int out_size, void* d_ws, size_t ws_size, hipStream_t stream) {
  Params P;
  for (int i = 0; i < 2; ++i) { P.x[i] = d_in[5 * i]; P.k[i] = d_in[5 * i + 1]; P.v[i] = d_in[5 * i + 2]; P.wq[i] = d_in[5 * i + 3]; P.wo[i] = d_in[5 * i + 4]; }
  P.out = d_out; P.ws = d_ws;
  fused_decode<<<256, 512, 0, stream>>>(P);
}
```

```cpp
#define AMP_RT(x) asm volatile("s_memrealtime %0\n\ts_waitcnt lgkmcnt(0)" : "=s"(x) :: "memory")
#define STAMP_DECL unsigned long long amp_t0 = 0, amp_t1 = 0;
#define STAMP(i) do { if ((i) == 0) { AMP_RT(amp_t0); amp_t1 = amp_t0; } else if ((i) == 8) { __builtin_amdgcn_sched_barrier(0); AMP_RT(amp_t1); __builtin_amdgcn_sched_barrier(0); } } while (0)
#define STAMP_FLUSH if (__builtin_amdgcn_readfirstlane(threadIdx.x >> 6) == 4 && (-1 < 0 || blockIdx.x == -1)) { unsigned long long d_ = amp_t1 - amp_t0; if (d_ > 5000) d_ = 5000; const unsigned long long until_ = amp_t0 + 3000 + 0 * d_; for (int i_ = 0; i_ < 4000000; ++i_) { unsigned long long n_; AMP_RT(n_); if (n_ >= until_) break; } }
#include <hip/hip_runtime.h>
#include <hip/hip_bf16.h>
#include <stdint.h>

typedef unsigned u32;
typedef unsigned long long u64;
typedef u32 v4u __attribute__((ext_vector_type(4)));
typedef u32 v2u __attribute__((ext_vector_type(2)));
typedef float v4f __attribute__((ext_vector_type(4)));
typedef float v2f __attribute__((ext_vector_type(2)));
typedef __bf16 v8b __attribute__((ext_vector_type(8)));
typedef __bf16 v2b __attribute__((ext_vector_type(2)));
typedef short v4s __attribute__((ext_vector_type(4)));
#define LAS __attribute__((address_space(3)))
typedef __amdgpu_buffer_rsrc_t rsrc_t;

constexpr int DM = 512, HQ = 32, HKV = 8, SK = 8192, DH = 128, WIN = 2048;
constexpr int AUX_NT = 2, AUX_SC1 = 16;
constexpr int WS_E = 0;
constexpr int WS_L = 4096;
constexpr int WS_Q = 16384;
constexpr int WS_O = WS_Q + 16 * 16384;
constexpr int WS_R = WS_O + 16 * 32768;
constexpr int WS_END = WS_R + 8 * 65536;
constexpr int L_CNT = 0;
constexpr int L_ML = 64;
constexpr int L_Q = 256;
constexpr int L_QP = L_Q + 1024;
constexpr int OP_W = 2 * 1088;
constexpr int L_OP = L_QP + 4096;
constexpr int L_RED = L_OP + 4 * OP_W;
constexpr int L_END = L_RED + 8192;
constexpr u32 SPIN_CAP = 1u << 20;

struct Params { const void* x[2]; const void* k[2]; const void* v[2]; const void* wq[2]; const void* wo[2]; void* out; void* ws; };

__device__ __forceinline__ rsrc_t mk(const void* p, int bytes) { return __builtin_amdgcn_make_buffer_rsrc((void*)p, (short)0, bytes, 0x00020000); }
template <int AUX> __device__ __forceinline__ v4u ld16(rsrc_t r, int voff, int soff) { return __builtin_amdgcn_raw_buffer_load_b128(r, voff, soff, AUX); }
__device__ __forceinline__ u32 pk(float lo, float hi) { v2f f = {lo, hi}; return __builtin_bit_cast(u32, __builtin_convertvector(f, v2b)); }
__device__ __forceinline__ float blo(u32 x) { return __builtin_bit_cast(float, x << 16); }
__device__ __forceinline__ float bhi(u32 x) { return __builtin_bit_cast(float, x & 0xffff0000u); }
__device__ __forceinline__ v4f mfma32(v4u a, v4u b, v4f c) { return __builtin_amdgcn_mfma_f32_16x16x32_bf16(__builtin_bit_cast(v8b, a), __builtin_bit_cast(v8b, b), c, 0, 0, 0); }
__device__ __forceinline__ v4f mfma16(v2u a, v2u b, v4f c) { return __builtin_amdgcn_mfma_f32_16x16x16bf16_1k(__builtin_bit_cast(v4s, a), __builtin_bit_cast(v4s, b), c, 0, 0, 0); }
template <int CTRL> __device__ __forceinline__ float dpp(float x) { return __builtin_bit_cast(float, __builtin_amdgcn_mov_dpp(__builtin_bit_cast(int, x), CTRL, 0xf, 0xf, true)); }
constexpr int ROR4 = 0x124, ROR8 = 0x128;
__device__ __forceinline__ float x16max(float x) { auto s = __builtin_amdgcn_permlane16_swap(__float_as_uint(x), __float_as_uint(x), false, false); return fmaxf(__uint_as_float(s[0]), __uint_as_float(s[1])); }
__device__ __forceinline__ float x32max(float x) { auto s = __builtin_amdgcn_permlane32_swap(__float_as_uint(x), __float_as_uint(x), false, false); return fmaxf(__uint_as_float(s[0]), __uint_as_float(s[1])); }
__device__ __forceinline__ float x16sum(float x) { auto s = __builtin_amdgcn_permlane16_swap(__float_as_uint(x), __float_as_uint(x), false, false); return __uint_as_float(s[0]) + __uint_as_float(s[1]); }
__device__ __forceinline__ float x32sum(float x) { auto s = __builtin_amdgcn_permlane32_swap(__float_as_uint(x), __float_as_uint(x), false, false); return __uint_as_float(s[0]) + __uint_as_float(s[1]); }
#define CBAR() asm volatile("" ::: "memory")
#define LGKM0() asm volatile("s_waitcnt lgkmcnt(0)" ::: "memory")
#define BARRIER() do { LGKM0(); __builtin_amdgcn_s_barrier(); CBAR(); } while (0)
#define SB() __builtin_amdgcn_sched_barrier(0)
#define WG_RLX __ATOMIC_RELAXED, __HIP_MEMORY_SCOPE_WORKGROUP

__global__ void __launch_bounds__(512, 2) fused_decode(Params P) {
  __shared__ __attribute__((aligned(16))) char smem[L_END];
  LAS char* lds = (LAS char*)smem;
  STAMP_DECL
  STAMP(0);
  const int blk = blockIdx.x;
  const int G = (blk & 7) * 2 + (blk >> 7), c16 = (blk >> 3) & 15;
  const int br = G & 1, g = G >> 1;
  const int lane = threadIdx.x & 63, wave = __builtin_amdgcn_readfirstlane(threadIdx.x >> 6);
  const int c = lane >> 4, n = lane & 15;
  const int b = c16 >> 3, sp = c16 & 7;
  const int hh = c16 & 3, qt = c16 >> 2;
  const rsrc_t rws = mk(P.ws, WS_END);
  LAS u32* cnt = (LAS u32*)(lds + L_CNT);

  if (wave < 4) {
    const int w = wave;
    const rsrc_t rx = mk(P.x[br], 2 * DM * 2), rq = mk(P.wq[br], DM * HQ * DH * 2);
    const int row0 = qt * 128 + w * 32 + c * 8;
    const v4u xr = ld16<0>(rx, ((n >> 3) * DM + row0) * 2, 0);
    v4u wv[8];
    const int qoff = row0 * (HQ * DH * 2) + ((g * 4 + hh) * DH + n * 8) * 2;
#pragma unroll
    for (int t = 0; t < 8; ++t) wv[t] = ld16<AUX_NT>(rq, qoff, t * (HQ * DH * 2));
    const u32 ev = __builtin_amdgcn_raw_buffer_load_b32(rws, 0, WS_E + br * 256, AUX_SC1);
    SB();
    BARRIER();
    STAMP(1);
    __builtin_amdgcn_s_setprio(2);
    const int j = n & 7;
    const u32 hm = (j & 1) ? 0xffff0000u : 0x0000ffffu;
    u32 mk4[4];
#pragma unroll
    for (int d = 0; d < 4; ++d) mk4[d] = (j >> 1) == d ? hm : 0u;
    v4f acc = {0.f, 0.f, 0.f, 0.f};
#pragma unroll
    for (int t = 0; t < 8; ++t) {
      const u32 xx = __builtin_amdgcn_perm(xr[t >> 1], xr[t >> 1], (t & 1) ? 0x03020302u : 0x01000100u);
      const v4u a = {xx & mk4[0], xx & mk4[1], xx & mk4[2], xx & mk4[3]};
      acc = mfma32(a, wv[t], acc);
    }
    const u32 tag = __builtin_amdgcn_readfirstlane(ev) + 1u;
    *(LAS v4f*)(lds + L_QP + w * 1024 + lane * 16) = acc;
    CBAR();
    u32 arr = 0;
    if (lane == 0) arr = __hip_atomic_fetch_add(cnt + 0, 1u, WG_RLX);
    arr = __builtin_amdgcn_readfirstlane(arr);
    CBAR();
    STAMP(2);
    if (arr == 3) {
      v4f s = *(LAS v4f*)(lds + L_QP + lane * 16);
#pragma unroll
      for (int i = 1; i < 4; ++i) s += *(LAS v4f*)(lds + L_QP + i * 1024 + lane * 16);
      const v4u m = {pk(s[0], s[1]), tag, pk(s[2], s[3]), tag};
      __builtin_amdgcn_raw_buffer_store_b128(m, rws, (c >> 1) * 8192 + n * 32 + (c & 1) * 16, WS_Q + G * 16384 + (hh * 4 + qt) * 512, AUX_SC1);
    }
    {
      const int so = WS_Q + G * 16384 + b * 8192 + w * 2048;
      v4u g0, g1;
      for (u32 spin = 0;;) {
        CBAR();
        g0 = ld16<AUX_SC1>(rws, lane * 16, so);
        g1 = ld16<AUX_SC1>(rws, lane * 16 + 1024, so);
        const bool ok = g0[1] == tag && g0[3] == tag && g1[1] == tag && g1[3] == tag;
        if (__all(ok) || ++spin > SPIN_CAP) break;
        __builtin_amdgcn_s_sleep(2);
      }
      const float q0 = x32sum(blo(g0[0]) + blo(g1[0])), q1 = x32sum(bhi(g0[0]) + bhi(g1[0]));
      const float q2 = x32sum(blo(g0[2]) + blo(g1[2])), q3 = x32sum(bhi(g0[2]) + bhi(g1[2]));
      if (lane < 32) *(LAS v2u*)(lds + L_Q + w * 256 + lane * 8) = v2u{pk(q0, q1), pk(q2, q3)};
      if (lane == 0) { cnt[3] = tag; CBAR(); __hip_atomic_fetch_add(cnt + 1, 1u, WG_RLX); }
      CBAR();
    }
    STAMP(3);
    __builtin_amdgcn_s_setprio(0);
    const rsrc_t ro = mk(P.wo[br], HQ * DH * DM * 2);
    const int ooff = ((g * 4 + hh) * DH + w * 32 + c * 8) * (DM * 2) + (qt * 128 + n * 8) * 2;
#pragma unroll
    for (int t = 0; t < 8; ++t) wv[t] = ld16<AUX_NT>(ro, ooff + (t & 3) * 1024, (t >> 2) * 4096);
    SB();
    STAMP(4);
    BARRIER();
    v4u bt[8];
#pragma unroll
    for (int d = 0; d < 4; ++d)
#pragma unroll
      for (int e = 0; e < 4; ++e) {
        bt[2 * d][e] = __builtin_amdgcn_perm(wv[2 * e + 1][d], wv[2 * e][d], 0x05040100u);
        bt[2 * d + 1][e] = __builtin_amdgcn_perm(wv[2 * e + 1][d], wv[2 * e][d], 0x07060302u);
      }
#pragma unroll
    for (int t = 0; t < 8; ++t) asm volatile("" : "+v"(bt[t]));
    SB();
    STAMP(5);
    v4u o0, o1, l0, l1;
    {
      const int so = WS_O + G * 32768 + hh * 8192 + w * 128, sl = WS_L + G * 512 + hh * 128;
      for (u32 spin = 0;;) {
        CBAR();
        o0 = ld16<AUX_SC1>(rws, n * 512 + c * 32, so);
        o1 = ld16<AUX_SC1>(rws, n * 512 + c * 32 + 16, so);
        l0 = ld16<AUX_SC1>(rws, c * 32, sl);
        l1 = ld16<AUX_SC1>(rws, c * 32 + 16, sl);
        const bool ok = o0[1] == tag && o0[3] == tag && o1[1] == tag && o1[3] == tag && l0[1] == tag && l0[3] == tag && l1[1] == tag && l1[3] == tag;
        if (__all(ok) || ++spin > SPIN_CAP) break;
      }
    }
    STAMP(6);
    const v4u oa = {o0[0], o0[2], o1[0], o1[2]};
    const float a0 = __uint_as_float(l0[0]), a1 = __uint_as_float(l0[2]), a2 = __uint_as_float(l1[0]), a3 = __uint_as_float(l1[2]);
    const float mx = x16max(fmaxf(fmaxf(a0, a1), fmaxf(a2, a3)));
    float w0 = __builtin_amdgcn_exp2f(a0 - mx), w1 = __builtin_amdgcn_exp2f(a1 - mx), w2 = __builtin_amdgcn_exp2f(a2 - mx), w3 = __builtin_amdgcn_exp2f(a3 - mx);
    const float inv = __builtin_amdgcn_rcpf(x16sum((w0 + w1) + (w2 + w3)));
    w0 *= inv; w1 *= inv; w2 *= inv; w3 *= inv;
    float e[8];
#pragma unroll
    for (int jj = 0; jj < 8; ++jj) {
      const v4f d = mfma32(oa, bt[jj], v4f{0.f, 0.f, 0.f, 0.f});
      e[jj] = fmaf(d[3], w3, fmaf(d[2], w2, fmaf(d[1], w1, d[0] * w0)));
    }
    *(LAS v4f*)(lds + L_RED + w * 2048 + c * 512 + n * 16) = v4f{e[0], e[1], e[2], e[3]};
    *(LAS v4f*)(lds + L_RED + w * 2048 + c * 512 + 256 + n * 16) = v4f{e[4], e[5], e[6], e[7]};
    BARRIER();
    STAMP(7);
    if (w < 2) {
      const int idx = ((lane >> 1) & 1) * 256 + (lane >> 2) * 16 + (lane & 1) * 8;
      v2f s = {0.f, 0.f};
#pragma unroll
      for (int i = 0; i < 8; ++i) s += *(LAS v2f*)(lds + L_RED + (i >> 1) * 2048 + (2 * w + (i & 1)) * 512 + idx);
      const v4u m = {__float_as_uint(s[0]), tag, __float_as_uint(s[1]), tag};
      __builtin_amdgcn_raw_buffer_store_b128(m, rws, (lane >> 2) * 2048 + (lane & 3) * 16, WS_R + (br * 4 + qt) * 65536 + w * 32768 + (g * 4 + hh) * 64, AUX_SC1);
    }
    STAMP(8);
  } else {
    const int w = wave - 4;
    if (w == 0 && lane < 4) cnt[lane] = 0u;
    const size_t kvbase = ((size_t)(b * HKV + g) * SK + (SK - WIN) + sp * 256) * (DH * 2);
    const rsrc_t rk = mk((const char*)P.k[br] + kvbase, 65536), rv = mk((const char*)P.v[br] + kvbase, 65536);
    const int koff = w * 16384 + ((lane >> 2) & 3) * 256 + c * 64 + (lane & 3) * 16;
    const int voff = w * 16384 + lane * 16;
    __builtin_amdgcn_s_setprio(1);
    BARRIER();
    STAMP(1);
    v4u kk[16], vv[16];
#pragma unroll
    for (int t = 0; t < 16; ++t) kk[t] = ld16<AUX_NT>(rk, koff + (t & 3) * 1024, (t >> 2) * 4096);
#pragma unroll
    for (int t = 0; t < 16; ++t) vv[t] = ld16<AUX_NT>(rv, voff + (t & 3) * 1024, (t >> 2) * 4096);
    SB();
    STAMP(2);
    for (u32 spin = 0; __builtin_amdgcn_readfirstlane(__hip_atomic_load(cnt + 1, WG_RLX)) != 4u && ++spin < SPIN_CAP * 16;) __builtin_amdgcn_s_sleep(1);
    CBAR();
    STAMP(3);
    const int u = (lane >> 2) & 3, h = lane & 3;
    const v4u qf = *(LAS v4u*)(lds + L_Q + h * 256 + (4 * c + u) * 16);
    const u32 tag = __builtin_amdgcn_readfirstlane(cnt[3]);
    constexpr float CS = 0.08838834764831845f * 1.4426950408889634f;
    float s[16];
#pragma unroll
    for (int t = 0; t < 16; ++t) {
      const v4f d = mfma32(kk[t], qf, v4f{0.f, 0.f, 0.f, 0.f});
      float x = (u & 2) ? ((u & 1) ? d[3] : d[2]) : ((u & 1) ? d[1] : d[0]);
      x += dpp<ROR8>(x);
      x += dpp<ROR4>(x);
      s[t] = x * CS;
    }
    float m = s[0];
#pragma unroll
    for (int t = 1; t < 16; ++t) m = fmaxf(m, s[t]);
    m = x32max(x16max(m));
    float l = 0.f;
#pragma unroll
    for (int t = 0; t < 16; ++t) { s[t] = __builtin_amdgcn_exp2f(s[t] - m); l += s[t]; }
    l = x32sum(x16sum(l));
    if (lane < 4) *(LAS v2f*)(lds + L_ML + w * 32 + lane * 8) = v2f{m, l};
    CBAR();
    if (lane == 0) __hip_atomic_fetch_add(cnt + 2, 1u, WG_RLX);
    for (u32 spin = 0; __builtin_amdgcn_readfirstlane(__hip_atomic_load(cnt + 2, WG_RLX)) != 4u && ++spin < SPIN_CAP * 16;) {}
    CBAR();
    v2f ml[4];
#pragma unroll
    for (int i = 0; i < 4; ++i) ml[i] = *(LAS v2f*)(lds + L_ML + i * 32 + h * 8);
    const float M = fmaxf(fmaxf(ml[0][0], ml[1][0]), fmaxf(ml[2][0], ml[3][0]));
    float L = 0.f;
#pragma unroll
    for (int i = 0; i < 4; ++i) L = fmaf(__builtin_amdgcn_exp2f(ml[i][0] - M), ml[i][1], L);
    const float f = __builtin_amdgcn_exp2f(m - M) * __builtin_amdgcn_rcpf(L);
    const float lse = M + __builtin_amdgcn_logf(L);
    const u32 hm = (u & 1) ? 0xffff0000u : 0x0000ffffu;
    const u32 m0 = (u & 2) ? 0u : hm, m1 = (u & 2) ? hm : 0u;
    v2u pa[16];
#pragma unroll
    for (int t = 0; t < 16; ++t) { const float p = s[t] * f; const u32 pp = pk(p, p); pa[t] = v2u{pp & m0, pp & m1}; }
#pragma unroll
    for (int t = 0; t < 16; ++t) asm volatile("" : "+v"(pa[t]));
    SB();
    STAMP(4);
    v4f d1 = {0.f, 0.f, 0.f, 0.f}, d2 = {0.f, 0.f, 0.f, 0.f};
#pragma unroll
    for (int t = 0; t < 16; ++t) {
      d1 = mfma16(pa[t], v2u{vv[t][0], vv[t][1]}, d1);
      d2 = mfma16(pa[t], v2u{vv[t][2], vv[t][3]}, d2);
    }
    *(LAS v4f*)(lds + L_OP + w * OP_W + lane * 16) = d1;
    *(LAS v4f*)(lds + L_OP + w * OP_W + 1088 + lane * 16) = d2;
    STAMP(5);
    BARRIER();
    {
      const int a = L_OP + ((n >> 1) & 1) * 1088 + (n & 1) * 512 + (4 * w + (n >> 2)) * 16 + c * 4;
      float s0 = 0.f, s1 = 0.f;
#pragma unroll
      for (int i = 0; i < 4; ++i) { s0 += *(LAS float*)(lds + a + i * OP_W); s1 += *(LAS float*)(lds + a + i * OP_W + 256); }
      __builtin_amdgcn_raw_buffer_store_b64(v2u{pk(s0, s1), tag}, rws, c * 8192 + n * 8, WS_O + G * 32768 + c16 * 512 + w * 128, AUX_SC1);
      if (w == 0 && lane < 4) __builtin_amdgcn_raw_buffer_store_b64(v2u{__float_as_uint(lse), tag}, rws, lane * 128, WS_L + G * 512 + c16 * 8, AUX_SC1);
    }
    STAMP(6);
    BARRIER();
    STAMP(7);
    if (w == 0) {
      const int r = g * 4 + hh;
      const int so = WS_R + (br * 4 + qt) * 65536 + r * 2048;
      v4u g0, g1;
      for (u32 spin = 0;;) {
        CBAR();
        g0 = ld16<AUX_SC1>(rws, lane * 16, so);
        g1 = ld16<AUX_SC1>(rws, lane * 16 + 1024, so);
        const bool ok = g0[1] == tag && g0[3] == tag && g1[1] == tag && g1[3] == tag;
        if (__all(ok) || ++spin > SPIN_CAP) break;
      }
      float y0 = __uint_as_float(g0[0]) + __uint_as_float(g1[0]), y1 = __uint_as_float(g0[2]) + __uint_as_float(g1[2]);
      y0 += dpp<ROR8>(y0); y1 += dpp<ROR8>(y1);
      y0 += dpp<ROR4>(y0); y1 += dpp<ROR4>(y1);
      y0 = x32sum(x16sum(y0)); y1 = x32sum(x16sum(y1));
      if (lane < 4) ((u32*)P.out)[(r >> 4) * 512 + br * 256 + qt * 64 + (r & 15) * 4 + lane] = pk(y0, y1);
      if (g == 0 && c16 == 0 && lane == 0) __builtin_amdgcn_raw_buffer_store_b32(tag, rws, 0, WS_E + br * 256, AUX_SC1);
    }
    STAMP(8);
  }
  STAMP_FLUSH
}

extern "C" void kernel_launch(void* const* d_in, const int* in_sizes, int n_in, void* d_out, int out_size, void* d_ws, size_t ws_size, hipStream_t stream) {
  Params P;
  for (int i = 0; i < 2; ++i) { P.x[i] = d_in[5 * i]; P.k[i] = d_in[5 * i + 1]; P.v[i] = d_in[5 * i + 2]; P.wq[i] = d_in[5 * i + 3]; P.wo[i] = d_in[5 * i + 4]; }
  P.out = d_out; P.ws = d_ws;
  fused_decode<<<256, 512, 0, stream>>>(P);
}
```

```cpp
#include <hip/hip_runtime.h>
#define AMP_RT(x) asm volatile("s_memrealtime %0\n\ts_waitcnt lgkmcnt(0)" : "=s"(x) :: "memory")
#define CAL_OFF (32u << 20)
#define STAMP_DECL unsigned long long amp_t0 = 0;
#define STAMP(i) do { if ((i) == 0) { AMP_RT(amp_t0); } } while (0)
#define CAL_WAIT(t0) { const unsigned long long until_ = (t0) + 4000; for (int i_ = 0; i_ < 100000; ++i_) { unsigned long long n_; AMP_RT(n_); if (n_ >= until_) break; } }
#define STAMP_FLUSH if (threadIdx.x == 256 && blockIdx.x == 0) { unsigned long long* p_ = (unsigned long long*)((char*)P.ws + CAL_OFF); \
   if (0) amp_t0 = *p_; else *p_ = amp_t0; if (!1) CAL_WAIT(amp_t0) }
__global__ void cal_first(unsigned long long* p) { unsigned long long t; AMP_RT(t); *p = t; }
__global__ void cal_last(unsigned long long* p) { unsigned long long t = *p; CAL_WAIT(t) }
#include <hip/hip_runtime.h>
#include <hip/hip_bf16.h>
#include <stdint.h>

typedef unsigned u32;
typedef unsigned long long u64;
typedef u32 v4u __attribute__((ext_vector_type(4)));
typedef u32 v2u __attribute__((ext_vector_type(2)));
typedef float v4f __attribute__((ext_vector_type(4)));
typedef float v2f __attribute__((ext_vector_type(2)));
typedef __bf16 v8b __attribute__((ext_vector_type(8)));
typedef __bf16 v2b __attribute__((ext_vector_type(2)));
typedef short v4s __attribute__((ext_vector_type(4)));
#define LAS __attribute__((address_space(3)))
typedef __amdgpu_buffer_rsrc_t rsrc_t;

constexpr int DM = 512, HQ = 32, HKV = 8, SK = 8192, DH = 128, WIN = 2048;
constexpr int AUX_NT = 2, AUX_SC1 = 16;
constexpr int WS_E = 0;
constexpr int WS_L = 4096;
constexpr int WS_Q = 16384;
constexpr int WS_O = WS_Q + 16 * 16384;
constexpr int WS_R = WS_O + 16 * 32768;
constexpr int WS_END = WS_R + 8 * 65536;
constexpr int L_CNT = 0;
constexpr int L_ML = 64;
constexpr int L_Q = 256;
constexpr int L_QP = L_Q + 1024;
constexpr int OP_W = 2 * 1088;
constexpr int L_OP = L_QP + 4096;
constexpr int L_RED = L_OP + 4 * OP_W;
constexpr int L_END = L_RED + 8192;
constexpr u32 SPIN_CAP = 1u << 20;

struct Params { const void* x[2]; const void* k[2]; const void* v[2]; const void* wq[2]; const void* wo[2]; void* out; void* ws; };

__device__ __forceinline__ rsrc_t mk(const void* p, int bytes) { return __builtin_amdgcn_make_buffer_rsrc((void*)p, (short)0, bytes, 0x00020000); }
template <int AUX> __device__ __forceinline__ v4u ld16(rsrc_t r, int voff, int soff) { return __builtin_amdgcn_raw_buffer_load_b128(r, voff, soff, AUX); }
__device__ __forceinline__ u32 pk(float lo, float hi) { v2f f = {lo, hi}; return __builtin_bit_cast(u32, __builtin_convertvector(f, v2b)); }
__device__ __forceinline__ float blo(u32 x) { return __builtin_bit_cast(float, x << 16); }
__device__ __forceinline__ float bhi(u32 x) { return __builtin_bit_cast(float, x & 0xffff0000u); }
__device__ __forceinline__ v4f mfma32(v4u a, v4u b, v4f c) { return __builtin_amdgcn_mfma_f32_16x16x32_bf16(__builtin_bit_cast(v8b, a), __builtin_bit_cast(v8b, b), c, 0, 0, 0); }
__device__ __forceinline__ v4f mfma16(v2u a, v2u b, v4f c) { return __builtin_amdgcn_mfma_f32_16x16x16bf16_1k(__builtin_bit_cast(v4s, a), __builtin_bit_cast(v4s, b), c, 0, 0, 0); }
template <int CTRL> __device__ __forceinline__ float dpp(float x) { return __builtin_bit_cast(float, __builtin_amdgcn_mov_dpp(__builtin_bit_cast(int, x), CTRL, 0xf, 0xf, true)); }
constexpr int ROR4 = 0x124, ROR8 = 0x128;
__device__ __forceinline__ float x16max(float x) { auto s = __builtin_amdgcn_permlane16_swap(__float_as_uint(x), __float_as_uint(x), false, false); return fmaxf(__uint_as_float(s[0]), __uint_as_float(s[1])); }
__device__ __forceinline__ float x32max(float x) { auto s = __builtin_amdgcn_permlane32_swap(__float_as_uint(x), __float_as_uint(x), false, false); return fmaxf(__uint_as_float(s[0]), __uint_as_float(s[1])); }
__device__ __forceinline__ float x16sum(float x) { auto s = __builtin_amdgcn_permlane16_swap(__float_as_uint(x), __float_as_uint(x), false, false); return __uint_as_float(s[0]) + __uint_as_float(s[1]); }
__device__ __forceinline__ float x32sum(float x) { auto s = __builtin_amdgcn_permlane32_swap(__float_as_uint(x), __float_as_uint(x), false, false); return __uint_as_float(s[0]) + __uint_as_float(s[1]); }
#define CBAR() asm volatile("" ::: "memory")
#define LGKM0() asm volatile("s_waitcnt lgkmcnt(0)" ::: "memory")
#define BARRIER() do { LGKM0(); __builtin_amdgcn_s_barrier(); CBAR(); } while (0)
#define SB() __builtin_amdgcn_sched_barrier(0)
#define WG_RLX __ATOMIC_RELAXED, __HIP_MEMORY_SCOPE_WORKGROUP

__global__ void __launch_bounds__(512, 2) fused_decode(Params P) {
  __shared__ __attribute__((aligned(16))) char smem[L_END];
  LAS char* lds = (LAS char*)smem;
  STAMP_DECL
  STAMP(0);
  const int blk = blockIdx.x;
  const int G = (blk & 7) * 2 + (blk >> 7), c16 = (blk >> 3) & 15;
  const int br = G & 1, g = G >> 1;
  const int lane = threadIdx.x & 63, wave = __builtin_amdgcn_readfirstlane(threadIdx.x >> 6);
  const int c = lane >> 4, n = lane & 15;
  const int b = c16 >> 3, sp = c16 & 7;
  const int hh = c16 & 3, qt = c16 >> 2;
  const rsrc_t rws = mk(P.ws, WS_END);
  LAS u32* cnt = (LAS u32*)(lds + L_CNT);

  if (wave < 4) {
    const int w = wave;
    const rsrc_t rx = mk(P.x[br], 2 * DM * 2), rq = mk(P.wq[br], DM * HQ * DH * 2);
    const int row0 = qt * 128 + w * 32 + c * 8;
    const v4u xr = ld16<0>(rx, ((n >> 3) * DM + row0) * 2, 0);
    v4u wv[8];
    const int qoff = row0 * (HQ * DH * 2) + ((g * 4 + hh) * DH + n * 8) * 2;
#pragma unroll
    for (int t = 0; t < 8; ++t) wv[t] = ld16<AUX_NT>(rq, qoff, t * (HQ * DH * 2));
    const u32 ev = __builtin_amdgcn_raw_buffer_load_b32(rws, 0, WS_E + br * 256, AUX_SC1);
    SB();
    BARRIER();
    STAMP(1);
    __builtin_amdgcn_s_setprio(2);
    const int j = n & 7;
    const u32 hm = (j & 1) ? 0xffff0000u : 0x0000ffffu;
    u32 mk4[4];
#pragma unroll
    for (int d = 0; d < 4; ++d) mk4[d] = (j >> 1) == d ? hm : 0u;
    v4f acc = {0.f, 0.f, 0.f, 0.f};
#pragma unroll
    for (int t = 0; t < 8; ++t) {
      const u32 xx = __builtin_amdgcn_perm(xr[t >> 1], xr[t >> 1], (t & 1) ? 0x03020302u : 0x01000100u);
      const v4u a = {xx & mk4[0], xx & mk4[1], xx & mk4[2], xx & mk4[3]};
      acc = mfma32(a, wv[t], acc);
    }
    const u32 tag = __builtin_amdgcn_readfirstlane(ev) + 1u;
    *(LAS v4f*)(lds + L_QP + w * 1024 + lane * 16) = acc;
    CBAR();
    u32 arr = 0;
    if (lane == 0) arr = __hip_atomic_fetch_add(cnt + 0, 1u, WG_RLX);
    arr = __builtin_amdgcn_readfirstlane(arr);
    CBAR();
    STAMP(2);
    if (arr == 3) {
      v4f s = *(LAS v4f*)(lds + L_QP + lane * 16);
#pragma unroll
      for (int i = 1; i < 4; ++i) s += *(LAS v4f*)(lds + L_QP + i * 1024 + lane * 16);
      const v4u m = {pk(s[0], s[1]), tag, pk(s[2], s[3]), tag};
      __builtin_amdgcn_raw_buffer_store_b128(m, rws, (c >> 1) * 8192 + n * 32 + (c & 1) * 16, WS_Q + G * 16384 + (hh * 4 + qt) * 512, AUX_SC1);
    }
    {
      const int so = WS_Q + G * 16384 + b * 8192 + w * 2048;
      v4u g0, g1;
      for (u32 spin = 0;;) {
        CBAR();
        g0 = ld16<AUX_SC1>(rws, lane * 16, so);
        g1 = ld16<AUX_SC1>(rws, lane * 16 + 1024, so);
        const bool ok = g0[1] == tag && g0[3] == tag && g1[1] == tag && g1[3] == tag;
        if (__all(ok) || ++spin > SPIN_CAP) break;
        __builtin_amdgcn_s_sleep(2);
      }
      const float q0 = x32sum(blo(g0[0]) + blo(g1[0])), q1 = x32sum(bhi(g0[0]) + bhi(g1[0]));
      const float q2 = x32sum(blo(g0[2]) + blo(g1[2])), q3 = x32sum(bhi(g0[2]) + bhi(g1[2]));
      if (lane < 32) *(LAS v2u*)(lds + L_Q + w * 256 + lane * 8) = v2u{pk(q0, q1), pk(q2, q3)};
      if (lane == 0) { cnt[3] = tag; CBAR(); __hip_atomic_fetch_add(cnt + 1, 1u, WG_RLX); }
      CBAR();
    }
    STAMP(3);
    __builtin_amdgcn_s_setprio(0);
    const rsrc_t ro = mk(P.wo[br], HQ * DH * DM * 2);
    const int ooff = ((g * 4 + hh) * DH + w * 32 + c * 8) * (DM * 2) + (qt * 128 + n * 8) * 2;
#pragma unroll
    for (int t = 0; t < 8; ++t) wv[t] = ld16<AUX_NT>(ro, ooff + (t & 3) * 1024, (t >> 2) * 4096);
    SB();
    STAMP(4);
    BARRIER();
    v4u bt[8];
#pragma unroll
    for (int d = 0; d < 4; ++d)
#pragma unroll
      for (int e = 0; e < 4; ++e) {
        bt[2 * d][e] = __builtin_amdgcn_perm(wv[2 * e + 1][d], wv[2 * e][d], 0x05040100u);
        bt[2 * d + 1][e] = __builtin_amdgcn_perm(wv[2 * e + 1][d], wv[2 * e][d], 0x07060302u);
      }
#pragma unroll
    for (int t = 0; t < 8; ++t) asm volatile("" : "+v"(bt[t]));
    SB();
    STAMP(5);
    v4u o0, o1, l0, l1;
    {
      const int so = WS_O + G * 32768 + hh * 8192 + w * 128, sl = WS_L + G * 512 + hh * 128;
      for (u32 spin = 0;;) {
        CBAR();
        o0 = ld16<AUX_SC1>(rws, n * 512 + c * 32, so);
        o1 = ld16<AUX_SC1>(rws, n * 512 + c * 32 + 16, so);
        l0 = ld16<AUX_SC1>(rws, c * 32, sl);
        l1 = ld16<AUX_SC1>(rws, c * 32 + 16, sl);
        const bool ok = o0[1] == tag && o0[3] == tag && o1[1] == tag && o1[3] == tag && l0[1] == tag && l0[3] == tag && l1[1] == tag && l1[3] == tag;
        if (__all(ok) || ++spin > SPIN_CAP) break;
      }
    }
    STAMP(6);
    const v4u oa = {o0[0], o0[2], o1[0], o1[2]};
    const float a0 = __uint_as_float(l0[0]), a1 = __uint_as_float(l0[2]), a2 = __uint_as_float(l1[0]), a3 = __uint_as_float(l1[2]);
    const float mx = x16max(fmaxf(fmaxf(a0, a1), fmaxf(a2, a3)));
    float w0 = __builtin_amdgcn_exp2f(a0 - mx), w1 = __builtin_amdgcn_exp2f(a1 - mx), w2 = __builtin_amdgcn_exp2f(a2 - mx), w3 = __builtin_amdgcn_exp2f(a3 - mx);
    const float inv = __builtin_amdgcn_rcpf(x16sum((w0 + w1) + (w2 + w3)));
    w0 *= inv; w1 *= inv; w2 *= inv; w3 *= inv;
    float e[8];
#pragma unroll
    for (int jj = 0; jj < 8; ++jj) {
      const v4f d = mfma32(oa, bt[jj], v4f{0.f, 0.f, 0.f, 0.f});
      e[jj] = fmaf(d[3], w3, fmaf(d[2], w2, fmaf(d[1], w1, d[0] * w0)));
    }
    *(LAS v4f*)(lds + L_RED + w * 2048 + c * 512 + n * 16) = v4f{e[0], e[1], e[2], e[3]};
    *(LAS v4f*)(lds + L_RED + w * 2048 + c * 512 + 256 + n * 16) = v4f{e[4], e[5], e[6], e[7]};
    BARRIER();
    STAMP(7);
    if (w < 2) {
      const int idx = ((lane >> 1) & 1) * 256 + (lane >> 2) * 16 + (lane & 1) * 8;
      v2f s = {0.f, 0.f};
#pragma unroll
      for (int i = 0; i < 8; ++i) s += *(LAS v2f*)(lds + L_RED + (i >> 1) * 2048 + (2 * w + (i & 1)) * 512 + idx);
      const v4u m = {__float_as_uint(s[0]), tag, __float_as_uint(s[1]), tag};
      __builtin_amdgcn_raw_buffer_store_b128(m, rws, (lane >> 2) * 2048 + (lane & 3) * 16, WS_R + (br * 4 + qt) * 65536 + w * 32768 + (g * 4 + hh) * 64, AUX_SC1);
    }
    STAMP(8);
  } else {
    const int w = wave - 4;
    if (w == 0 && lane < 4) cnt[lane] = 0u;
    const size_t kvbase = ((size_t)(b * HKV + g) * SK + (SK - WIN) + sp * 256) * (DH * 2);
    const rsrc_t rk = mk((const char*)P.k[br] + kvbase, 65536), rv = mk((const char*)P.v[br] + kvbase, 65536);
    const int koff = w * 16384 + ((lane >> 2) & 3) * 256 + c * 64 + (lane & 3) * 16;
    const int voff = w * 16384 + lane * 16;
    __builtin_amdgcn_s_setprio(1);
    BARRIER();
    STAMP(1);
    v4u kk[16], vv[16];
#pragma unroll
    for (int t = 0; t < 16; ++t) kk[t] = ld16<AUX_NT>(rk, koff + (t & 3) * 1024, (t >> 2) * 4096);
#pragma unroll
    for (int t = 0; t < 16; ++t) vv[t] = ld16<AUX_NT>(rv, voff + (t & 3) * 1024, (t >> 2) * 4096);
    SB();
    STAMP(2);
    for (u32 spin = 0; __builtin_amdgcn_readfirstlane(__hip_atomic_load(cnt + 1, WG_RLX)) != 4u && ++spin < SPIN_CAP * 16;) __builtin_amdgcn_s_sleep(1);
    CBAR();
    STAMP(3);
    const int u = (lane >> 2) & 3, h = lane & 3;
    const v4u qf = *(LAS v4u*)(lds + L_Q + h * 256 + (4 * c + u) * 16);
    const u32 tag = __builtin_amdgcn_readfirstlane(cnt[3]);
    constexpr float CS = 0.08838834764831845f * 1.4426950408889634f;
    float s[16];
#pragma unroll
    for (int t = 0; t < 16; ++t) {
      const v4f d = mfma32(kk[t], qf, v4f{0.f, 0.f, 0.f, 0.f});
      float x = (u & 2) ? ((u & 1) ? d[3] : d[2]) : ((u & 1) ? d[1] : d[0]);
      x += dpp<ROR8>(x);
      x += dpp<ROR4>(x);
      s[t] = x * CS;
    }
    float m = s[0];
#pragma unroll
    for (int t = 1; t < 16; ++t) m = fmaxf(m, s[t]);
    m = x32max(x16max(m));
    float l = 0.f;
#pragma unroll
    for (int t = 0; t < 16; ++t) { s[t] = __builtin_amdgcn_exp2f(s[t] - m); l += s[t]; }
    l = x32sum(x16sum(l));
    if (lane < 4) *(LAS v2f*)(lds + L_ML + w * 32 + lane * 8) = v2f{m, l};
    CBAR();
    if (lane == 0) __hip_atomic_fetch_add(cnt + 2, 1u, WG_RLX);
    for (u32 spin = 0; __builtin_amdgcn_readfirstlane(__hip_atomic_load(cnt + 2, WG_RLX)) != 4u && ++spin < SPIN_CAP * 16;) {}
    CBAR();
    v2f ml[4];
#pragma unroll
    for (int i = 0; i < 4; ++i) ml[i] = *(LAS v2f*)(lds + L_ML + i * 32 + h * 8);
    const float M = fmaxf(fmaxf(ml[0][0], ml[1][0]), fmaxf(ml[2][0], ml[3][0]));
    float L = 0.f;
#pragma unroll
    for (int i = 0; i < 4; ++i) L = fmaf(__builtin_amdgcn_exp2f(ml[i][0] - M), ml[i][1], L);
    const float f = __builtin_amdgcn_exp2f(m - M) * __builtin_amdgcn_rcpf(L);
    const float lse = M + __builtin_amdgcn_logf(L);
    const u32 hm = (u & 1) ? 0xffff0000u : 0x0000ffffu;
    const u32 m0 = (u & 2) ? 0u : hm, m1 = (u & 2) ? hm : 0u;
    v2u pa[16];
#pragma unroll
    for (int t = 0; t < 16; ++t) { const float p = s[t] * f; const u32 pp = pk(p, p); pa[t] = v2u{pp & m0, pp & m1}; }
#pragma unroll
    for (int t = 0; t < 16; ++t) asm volatile("" : "+v"(pa[t]));
    SB();
    STAMP(4);
    v4f d1 = {0.f, 0.f, 0.f, 0.f}, d2 = {0.f, 0.f, 0.f, 0.f};
#pragma unroll
    for (int t = 0; t < 16; ++t) {
      d1 = mfma16(pa[t], v2u{vv[t][0], vv[t][1]}, d1);
      d2 = mfma16(pa[t], v2u{vv[t][2], vv[t][3]}, d2);
    }
    *(LAS v4f*)(lds + L_OP + w * OP_W + lane * 16) = d1;
    *(LAS v4f*)(lds + L_OP + w * OP_W + 1088 + lane * 16) = d2;
    STAMP(5);
    BARRIER();
    {
      const int a = L_OP + ((n >> 1) & 1) * 1088 + (n & 1) * 512 + (4 * w + (n >> 2)) * 16 + c * 4;
      float s0 = 0.f, s1 = 0.f;
#pragma unroll
      for (int i = 0; i < 4; ++i) { s0 += *(LAS float*)(lds + a + i * OP_W); s1 += *(LAS float*)(lds + a + i * OP_W + 256); }
      __builtin_amdgcn_raw_buffer_store_b64(v2u{pk(s0, s1), tag}, rws, c * 8192 + n * 8, WS_O + G * 32768 + c16 * 512 + w * 128, AUX_SC1);
      if (w == 0 && lane < 4) __builtin_amdgcn_raw_buffer_store_b64(v2u{__float_as_uint(lse), tag}, rws, lane * 128, WS_L + G * 512 + c16 * 8, AUX_SC1);
    }
    STAMP(6);
    BARRIER();
    STAMP(7);
    if (w == 0) {
      const int r = g * 4 + hh;
      const int so = WS_R + (br * 4 + qt) * 65536 + r * 2048;
      v4u g0, g1;
      for (u32 spin = 0;;) {
        CBAR();
        g0 = ld16<AUX_SC1>(rws, lane * 16, so);
        g1 = ld16<AUX_SC1>(rws, lane * 16 + 1024, so);
        const bool ok = g0[1] == tag && g0[3] == tag && g1[1] == tag && g1[3] == tag;
        if (__all(ok) || ++spin > SPIN_CAP) break;
      }
      float y0 = __uint_as_float(g0[0]) + __uint_as_float(g1[0]), y1 = __uint_as_float(g0[2]) + __uint_as_float(g1[2]);
      y0 += dpp<ROR8>(y0); y1 += dpp<ROR8>(y1);
      y0 += dpp<ROR4>(y0); y1 += dpp<ROR4>(y1);
      y0 = x32sum(x16sum(y0)); y1 = x32sum(x16sum(y1));
      if (lane < 4) ((u32*)P.out)[(r >> 4) * 512 + br * 256 + qt * 64 + (r & 15) * 4 + lane] = pk(y0, y1);
      if (g == 0 && c16 == 0 && lane == 0) __builtin_amdgcn_raw_buffer_store_b32(tag, rws, 0, WS_E + br * 256, AUX_SC1);
    }
    STAMP(8);
  }
  STAMP_FLUSH
}

extern "C" void kernel_launch(void* const* d_in, const int* in_sizes, int n_in, void* d_out, int out_size, void* d_ws, size_t ws_size, hipStream_t stream) {
  Params P;
  for (int i = 0; i < 2; ++i) { P.x[i] = d_in[5 * i]; P.k[i] = d_in[5 * i + 1]; P.v[i] = d_in[5 * i + 2]; P.wq[i] = d_in[5 * i + 3]; P.wo[i] = d_in[5 * i + 4]; }
  P.out = d_out; P.ws = d_ws;
  fused_decode<<<256, 512, 0, stream>>>(P);
  cal_last<<<1, 64, 0, stream>>>((unsigned long long*)((char*)d_ws + CAL_OFF));
}
```

```cpp
#include <hip/hip_runtime.h>
#include <hip/hip_bf16.h>
#include <stdint.h>

typedef unsigned u32;
typedef unsigned long long u64;
typedef u32 v4u __attribute__((ext_vector_type(4)));
typedef u32 v2u __attribute__((ext_vector_type(2)));
typedef float v4f __attribute__((ext_vector_type(4)));
typedef float v2f __attribute__((ext_vector_type(2)));
typedef __bf16 v8b __attribute__((ext_vector_type(8)));
typedef __bf16 v2b __attribute__((ext_vector_type(2)));
typedef short v4s __attribute__((ext_vector_type(4)));
#define LAS __attribute__((address_space(3)))
typedef __amdgpu_buffer_rsrc_t rsrc_t;

#define STAMP(i)
#define STAMP_DECL
#define STAMP_FLUSH
#define DK 4
#define DV 4
#define DO 4
#define YSTART 15
#define QPOS 11
#define SLPQ 4
#define OPOS 7
#define TOUCH 0
#define SLP2 6
#define SLP3 8

constexpr int DM = 512, HQ = 32, HKV = 8, SK = 8192, DH = 128, WIN = 2048;
constexpr int AUX_NT = 2, AUX_SC1 = 16;
constexpr int WS_E = 0;
constexpr int WS_L = 4096;
constexpr int WS_Q = WS_L + 32768;
constexpr int O_H = 8192 + 512, O_G = 4 * O_H;
constexpr int WS_O = WS_Q + 16 * 16384;
constexpr int R_P = 2048 + 128, R_C = 32 * R_P;
constexpr int WS_R = WS_O + 16 * O_G;
constexpr int WS_END = WS_R + 8 * R_C;
constexpr int L_CNT = 0;
constexpr int L_ML = 64;
constexpr int L_WT = 192;
constexpr int L_Q = 256;
constexpr int L_P = L_Q + 1024;
constexpr int L_QP = L_P + 2048;
constexpr int OP_W = 2 * 1088;
constexpr int L_OP = L_QP + 4096;
constexpr int L_OA = L_OP + 4 * OP_W;
constexpr int L_RED = L_OA + 4 * 1280;
constexpr int L_END = L_RED + 8192;
constexpr u32 SPIN_CAP = 1u << 14;

struct Side { const void* x; const void* wq; const void* k; const void* v; const void* wo; void* out; void* ws; void* unused; };
struct Params { Side s[2]; };

__device__ __forceinline__ rsrc_t mk(const void* p, int bytes) { return __builtin_amdgcn_make_buffer_rsrc((void*)p, (short)0, bytes, 0x00020000); }
template <int AUX> __device__ __forceinline__ v4u ld16(rsrc_t r, int voff, int soff) { return __builtin_amdgcn_raw_buffer_load_b128(r, voff, soff, AUX); }
__device__ __forceinline__ u32 pk(float lo, float hi) { v2f f = {lo, hi}; return __builtin_bit_cast(u32, __builtin_convertvector(f, v2b)); }
__device__ __forceinline__ float blo(u32 x) { return __builtin_bit_cast(float, x << 16); }
__device__ __forceinline__ float bhi(u32 x) { return __builtin_bit_cast(float, x & 0xffff0000u); }
__device__ __forceinline__ v4f mfma32(v4u a, v4u b, v4f c) { return __builtin_amdgcn_mfma_f32_16x16x32_bf16(__builtin_bit_cast(v8b, a), __builtin_bit_cast(v8b, b), c, 0, 0, 0); }
__device__ __forceinline__ v4f mfma16(v2u a, v2u b, v4f c) { return __builtin_amdgcn_mfma_f32_16x16x16bf16_1k(__builtin_bit_cast(v4s, a), __builtin_bit_cast(v4s, b), c, 0, 0, 0); }
template <int CTRL> __device__ __forceinline__ float dpp(float x) { return __builtin_bit_cast(float, __builtin_amdgcn_mov_dpp(__builtin_bit_cast(int, x), CTRL, 0xf, 0xf, true)); }
constexpr int ROR4 = 0x124, ROR8 = 0x128, ROR12 = 0x12c, XOR1 = 0xB1, XOR2 = 0x4E, XOR7 = 0x141;
__device__ __forceinline__ float x16max(float x) { auto s = __builtin_amdgcn_permlane16_swap(__float_as_uint(x), __float_as_uint(x), false, false); return fmaxf(__uint_as_float(s[0]), __uint_as_float(s[1])); }
__device__ __forceinline__ float x32max(float x) { auto s = __builtin_amdgcn_permlane32_swap(__float_as_uint(x), __float_as_uint(x), false, false); return fmaxf(__uint_as_float(s[0]), __uint_as_float(s[1])); }
__device__ __forceinline__ float x16sum(float x) { auto s = __builtin_amdgcn_permlane16_swap(__float_as_uint(x), __float_as_uint(x), false, false); return __uint_as_float(s[0]) + __uint_as_float(s[1]); }
__device__ __forceinline__ float x32sum(float x) { auto s = __builtin_amdgcn_permlane32_swap(__float_as_uint(x), __float_as_uint(x), false, false); return __uint_as_float(s[0]) + __uint_as_float(s[1]); }
#define CBAR() asm volatile("" ::: "memory")
#define LGKM0() asm volatile("s_waitcnt lgkmcnt(0)" ::: "memory")
#define BARRIER() do { LGKM0(); __builtin_amdgcn_s_barrier(); CBAR(); } while (0)
#define SB() __builtin_amdgcn_sched_barrier(0)
#define LANDED(x) do { SB(); asm volatile("" : "+v"(x)); SB(); } while (0)
#define WG_RLX __ATOMIC_RELAXED, __HIP_MEMORY_SCOPE_WORKGROUP
__device__ __forceinline__ void lds_wait(LAS u32* p, u32 want, bool nap) {
  for (u32 spin = 0; __builtin_amdgcn_readfirstlane(__hip_atomic_load(p, WG_RLX)) != want && ++spin < SPIN_CAP * 16;) if (nap) __builtin_amdgcn_s_sleep(1);
  CBAR();
}
__device__ __forceinline__ void lds_arrive(LAS u32* p) { CBAR(); __hip_atomic_fetch_add(p, 1u, WG_RLX); CBAR(); }

__global__ void __launch_bounds__(512, 2) fused_decode(Params PP) {
  __shared__ __attribute__((aligned(16))) char smem[L_END];
  LAS char* lds = (LAS char*)smem;
  STAMP_DECL
  STAMP(0);
  const int blk = blockIdx.x;
  const int br = blk >> 7, g = blk & 7, c16 = (blk >> 3) & 15;
  const int G = g * 2 + br;
  typedef u32 v16u __attribute__((ext_vector_type(16)));
  v16u ka;
  asm volatile("s_load_dwordx16 %0, %1, %2" : "=s"(ka) : "s"(__builtin_amdgcn_kernarg_segment_ptr()), "s"(br * 64));
  const int lane = threadIdx.x & 63, wave = __builtin_amdgcn_readfirstlane(threadIdx.x >> 6);
  const int c = lane >> 4, n = lane & 15;
  const int b = c16 >> 3, sp = c16 & 7;
  const int hh = c16 & 3, qt = c16 >> 2;
  const int w = wave & 3;
  const int kvoff = ((b * HKV + g) * SK + (SK - WIN) + sp * 256 + w * 64) * (DH * 2);
  LAS u32* cnt = (LAS u32*)(lds + L_CNT);
  const int row0 = qt * 128 + w * 32 + c * 8;
  int qoff = row0 * (HQ * DH * 2) + ((g * 4 + hh) * DH + n * 8) * 2;
  const int u = (lane >> 2) & 3, h = lane & 3;
  int koff = kvoff + u * 256 + c * 64 + h * 16;
  int xoff = ((n >> 3) * DM + row0) * 2;
  asm volatile("s_waitcnt lgkmcnt(0)" : "+s"(ka), "+v"(qoff), "+v"(koff), "+v"(xoff));
  Side P;
#define KA(i) (void*)(((u64)ka[2 * (i) + 1] << 32) | ka[2 * (i)])
  P.x = KA(0); P.wq = KA(1); P.k = KA(2); P.v = KA(3); P.wo = KA(4); P.out = KA(5); P.ws = KA(6);
  const rsrc_t rws = mk(P.ws, WS_END);
  u32 ev;
  asm volatile("s_load_dword %0, %1, %2" : "=&s"(ev) : "s"(P.ws), "s"(WS_E + br * 256));
  if (TOUCH && wave == 4) {
    u32 t0, t1, t2, t3;
    asm volatile("s_load_dword %0, %4, %8\n\ts_load_dword %1, %5, %8\n\ts_load_dword %2, %6, %9\n\ts_load_dword %3, %7, 0x0\n\ts_waitcnt lgkmcnt(0)"
                 : "=&s"(t0), "=&s"(t1), "=&s"(t2), "=&s"(t3) : "s"(P.k), "s"(P.v), "s"(P.wo), "s"(P.out), "s"(kvoff), "s"((g * 4 + hh) * DH * DM * 2));
  }

  if (__builtin_expect(wave < 4, 1)) {
    const rsrc_t rx = mk(P.x, 2 * DM * 2), rq = mk(P.wq, DM * HQ * DH * 2), rk = mk(P.k, 2 * HKV * SK * DH * 2);
    const v4u xr = ld16<0>(rx, xoff, 0);
    v4u wv[8], kk[16];
#pragma unroll
    for (int t = 0; t < 8; ++t) wv[t] = ld16<AUX_NT>(rq, qoff, t * (HQ * DH * 2));
    SB();
#pragma unroll
    for (int t = 0; t < DK; ++t) { kk[t] = ld16<AUX_NT>(rk, koff + (t & 3) * 1024, (t >> 2) * 4096); SB(); }
    BARRIER();
    asm volatile("" : "+s"(ev));
    const u32 tag = ev + 1u;
    STAMP(1);
    {
      const int j = n & 7;
      const u32 hm = (j & 1) ? 0xffff0000u : 0x0000ffffu;
      u32 mk4[4];
#pragma unroll
      for (int d = 0; d < 4; ++d) mk4[d] = (j >> 1) == d ? hm : 0u;
      v4u a[8];
#pragma unroll
      for (int t = 0; t < 8; ++t) {
        const u32 xx = __builtin_amdgcn_perm(xr[t >> 1], xr[t >> 1], (t & 1) ? 0x03020302u : 0x01000100u);
        a[t] = v4u{xx & mk4[0], xx & mk4[1], xx & mk4[2], xx & mk4[3]};
        asm volatile("" : "+v"(a[t]));
      }
      SB();
      v4f acc = {0.f, 0.f, 0.f, 0.f};
#pragma unroll
      for (int t = 0; t < 8; ++t) acc = mfma32(a[t], wv[t], acc);
      *(LAS v4f*)(lds + L_QP + w * 1024 + lane * 16) = acc;
    }
    lds_arrive(cnt + 0);
    STAMP(2);
#pragma unroll
    for (int t = DK; t < 16; ++t) {
      LANDED(kk[t - DK]);
      kk[t] = ld16<AUX_NT>(rk, koff + (t & 3) * 1024, (t >> 2) * 4096);
      if (t == YSTART) { SB(); cnt[4 + w] = 1u; }
      SB();
    }
    if (QPOS >= 16 - DK) LANDED(kk[QPOS]);
    STAMP(3);
    {
      const int so = WS_Q + G * 16384 + b * 8192 + w * 2048;
      v4u g0, g1;
      for (u32 spin = 0;;) {
        CBAR();
        g0 = ld16<AUX_SC1>(rws, lane * 16, so);
        g1 = ld16<AUX_SC1>(rws, lane * 16 + 1024, so);
        const bool ok = g0[1] == tag && g0[3] == tag && g1[1] == tag && g1[3] == tag;
        if (__all(ok) || ++spin > SPIN_CAP) break;
        __builtin_amdgcn_s_sleep(SLPQ);
      }
      const float q0 = x32sum(blo(g0[0]) + blo(g1[0])), q1 = x32sum(bhi(g0[0]) + bhi(g1[0]));
      const float q2 = x32sum(blo(g0[2]) + blo(g1[2])), q3 = x32sum(bhi(g0[2]) + bhi(g1[2]));
      *(LAS v2u*)(lds + L_Q + w * 256 + (lane & 31) * 8) = v2u{pk(q0, q1), pk(q2, q3)};
      lds_arrive(cnt + 1);
      lds_wait(cnt + 1, 256u, false);
    }
    STAMP(4);
    {
      const v4u qf = *(LAS v4u*)(lds + L_Q + h * 256 + (4 * c + u) * 16);
      constexpr float CS = 0.08838834764831845f * 1.4426950408889634f;
      float s[4];
      v4f d[16];
#pragma unroll
      for (int t = 0; t < 16; ++t) d[t] = mfma32(kk[t], qf, v4f{0.f, 0.f, 0.f, 0.f});
      SB();
      float a[16];
#pragma unroll
      for (int t = 0; t < 16; ++t) {
        const int be = t >> 2;
        a[t] = (d[t][be] + dpp<ROR8>(d[t][be ^ 2])) + dpp<ROR4>(d[t][(be + 3) & 3]) + dpp<ROR12>(d[t][(be + 1) & 3]);
      }
#pragma unroll
      for (int t = 0; t < 16; ++t) asm volatile("" : "+v"(a[t]));
#pragma unroll
      for (int T = 0; T < 4; ++T) s[T] = ((u & 2) ? ((u & 1) ? a[12 + T] : a[8 + T]) : ((u & 1) ? a[4 + T] : a[T])) * CS;
      float m = fmaxf(fmaxf(s[0], s[1]), fmaxf(s[2], s[3]));
      m = fmaxf(m, dpp<ROR8>(m)); m = fmaxf(m, dpp<ROR4>(m));
      m = x32max(x16max(m));
#pragma unroll
      for (int T = 0; T < 4; ++T) s[T] = __builtin_amdgcn_exp2f(s[T] - m);
      float l = (s[0] + s[1]) + (s[2] + s[3]);
      l += dpp<ROR8>(l); l += dpp<ROR4>(l);
      l = x32sum(x16sum(l));
      *(LAS v2f*)(lds + L_ML + w * 32 + h * 8) = v2f{m, l};
      lds_arrive(cnt + 2);
      lds_wait(cnt + 2, 256u, false);
      v2f ml[4];
#pragma unroll
      for (int i = 0; i < 4; ++i) ml[i] = *(LAS v2f*)(lds + L_ML + i * 32 + h * 8);
      const float M = fmaxf(fmaxf(ml[0][0], ml[1][0]), fmaxf(ml[2][0], ml[3][0]));
      float L = 0.f;
#pragma unroll
      for (int i = 0; i < 4; ++i) L = fmaf(__builtin_amdgcn_exp2f(ml[i][0] - M), ml[i][1], L);
      const float f = __builtin_amdgcn_exp2f(m - M) * __builtin_amdgcn_rcpf(L);
      *(LAS v2u*)(lds + L_P + w * 512 + c * 128 + h * 32 + u * 8) = v2u{pk(s[0] * f, s[1] * f), pk(s[2] * f, s[3] * f)};
      CBAR();
      cnt[8 + w] = 1u;
      CBAR();
      if (w == 0 && lane < 4) __builtin_amdgcn_raw_buffer_store_b64(v2u{__float_as_uint(M + __builtin_amdgcn_logf(L)), tag}, rws, lane * 8, WS_L + (G * 16 + c16) * 128, AUX_SC1);
    }
    STAMP(5);
    int aop = L_OP + ((n >> 1) & 1) * 1088 + (n & 1) * 512 + (4 * w + (n >> 2)) * 16 + c * 4;
    int aos = c * O_H + n * 8;
    int ard = L_RED + (w >> 1) * 1024 + ((lane >> 2) & 1) * 256 + ((w & 1) * 8 + (lane >> 3)) * 16 + (lane & 3) * 4;
    int arg = (lane >> 2) * R_P + (lane & 3) * 16;
    asm volatile("" : "+v"(aop), "+v"(aos), "+v"(ard), "+v"(arg));
    SB();
    BARRIER();
    {
      float s0 = *(LAS float*)(lds + aop), s1 = *(LAS float*)(lds + aop + 256);
#pragma unroll
      for (int i = 1; i < 4; ++i) { s0 += *(LAS float*)(lds + aop + i * OP_W); s1 += *(LAS float*)(lds + aop + i * OP_W + 256); }
      __builtin_amdgcn_raw_buffer_store_b64(v2u{pk(s0, s1), tag}, rws, aos, WS_O + G * O_G + c16 * 512 + w * 128, AUX_SC1);
    }
    STAMP(6);
    if (w == 0) {
      v2u x;
      for (u32 spin = 0;;) {
        CBAR();
        x = __builtin_amdgcn_raw_buffer_load_b64(rws, lane < 16 ? lane * 128 : WS_END, WS_L + G * 2048 + hh * 8, AUX_SC1);
        if (__all(lane >= 16 || x[1] == tag) || ++spin > SPIN_CAP) break;
      }
      const float a = __uint_as_float(x[0]);
      float mx = fmaxf(a, dpp<XOR1>(a)); mx = fmaxf(mx, dpp<XOR2>(mx)); mx = fmaxf(mx, dpp<XOR7>(mx));
      const float e = __builtin_amdgcn_exp2f(a - mx);
      float z = e + dpp<XOR1>(e); z += dpp<XOR2>(z); z += dpp<XOR7>(z);
      if (lane < 16) *(LAS float*)(lds + L_WT + lane * 4) = e * __builtin_amdgcn_rcpf(z);
      CBAR();
      cnt[3] = 1u;
    }
    STAMP(7);
    BARRIER();
    {
      float s = *(LAS float*)(lds + ard);
#pragma unroll
      for (int i = 1; i < 8; ++i) s += *(LAS float*)(lds + ard + (i >> 1) * 2048 + (i & 1) * 512);
      __builtin_amdgcn_raw_buffer_store_b64(v2u{__float_as_uint(s), tag}, rws, lane * 8, WS_R + (br * 4 + qt) * R_C + (g * 4 + hh) * R_P + w * 512, AUX_SC1);
    }
    STAMP(8);
    if (w == 0) {
      __builtin_amdgcn_s_sleep(SLP3);
      const int r = g * 4 + hh;
      const int so = WS_R + (br * 4 + qt) * R_C + r * 64;
      v4u g0, g1;
      for (u32 spin = 0;;) {
        CBAR();
        g0 = ld16<AUX_SC1>(rws, arg, so);
        g1 = ld16<AUX_SC1>(rws, arg, so + 16 * R_P);
        const bool ok = g0[1] == tag && g0[3] == tag && g1[1] == tag && g1[3] == tag;
        if (__all(ok) || ++spin > SPIN_CAP) break;
      }
      float y0 = __uint_as_float(g0[0]) + __uint_as_float(g1[0]), y1 = __uint_as_float(g0[2]) + __uint_as_float(g1[2]);
      y0 += dpp<ROR8>(y0); y1 += dpp<ROR8>(y1);
      y0 += dpp<ROR4>(y0); y1 += dpp<ROR4>(y1);
      y0 = x32sum(x16sum(y0)); y1 = x32sum(x16sum(y1));
      if (lane < 4) ((__attribute__((address_space(1))) u32*)P.out)[(r >> 4) * 512 + br * 256 + qt * 64 + (r & 15) * 4 + lane] = pk(y0, y1);
      if (g == 0 && c16 == 0 && lane == 0) __builtin_amdgcn_raw_buffer_store_b32(tag, rws, 0, WS_E + br * 256, AUX_SC1);
    }
    STAMP(9);
  } else {
    if (w == 0 && lane < 16) cnt[lane] = 0u;
    const rsrc_t rv = mk(P.v, 2 * HKV * SK * DH * 2), ro = mk(P.wo, HQ * DH * DM * 2);
    const int voff = kvoff + lane * 16;
    const int ooff = ((g * 4 + hh) * DH + w * 32 + c * 8) * (DM * 2) + (qt * 128 + n * 8) * 2;
    const u32 slo = (u & 1) ? 0x01000c0cu : 0x0c0c0100u, shi = (u & 1) ? 0x03020c0cu : 0x0c0c0302u;
    const u32 s0l = (u & 2) ? 0x0c0c0c0cu : slo, s1l = (u & 2) ? slo : 0x0c0c0c0cu, s0h = (u & 2) ? 0x0c0c0c0cu : shi, s1h = (u & 2) ? shi : 0x0c0c0c0cu;
    BARRIER();
    asm volatile("" : "+s"(ev));
    const u32 tag = ev + 1u;
    if (w == 0) {
      const int a = L_QP + (((lane >> 5) * 2 + (lane & 1)) * 16 + ((lane & 31) >> 1)) * 16;
      lds_wait(cnt + 0, 256u, false);
      v4f s = *(LAS v4f*)(lds + a);
#pragma unroll
      for (int i = 1; i < 4; ++i) s += *(LAS v4f*)(lds + a + i * 1024);
      const v4u m = {pk(s[0], s[1]), tag, pk(s[2], s[3]), tag};
      __builtin_amdgcn_raw_buffer_store_b128(m, rws, (lane >> 5) * 8192 + (lane & 31) * 16, WS_Q + G * 16384 + (hh * 4 + qt) * 512, AUX_SC1);
    }
    STAMP(1);
    lds_wait(cnt + 4 + w, 1u, true);
    STAMP(2);
    v4u vv[16], wv[8];
#pragma unroll
    for (int t = 0; t < 16; ++t) {
      if (t >= DV) LANDED(vv[t - DV]);
      vv[t] = ld16<AUX_NT>(rv, voff + (t & 3) * 1024, (t >> 2) * 4096);
      SB();
    }
    STAMP(3);
    lds_wait(cnt + 8 + w, 1u, false);
    STAMP(4);
    const v4u p0 = *(LAS v4u*)(lds + L_P + w * 512 + c * 128 + h * 32), p1 = *(LAS v4u*)(lds + L_P + w * 512 + c * 128 + h * 32 + 16);
    v2u pa[16];
#pragma unroll
    for (int t = 0; t < 16; ++t) {
      const u32 pp = t < 8 ? p0[(t >> 1) & 3] : p1[(t >> 1) & 3];
      pa[t] = v2u{__builtin_amdgcn_perm(pp, pp, (t & 1) ? s0h : s0l), __builtin_amdgcn_perm(pp, pp, (t & 1) ? s1h : s1l)};
    }
    v4f d1 = {0.f, 0.f, 0.f, 0.f}, d2 = {0.f, 0.f, 0.f, 0.f};
#pragma unroll
    for (int t = 0; t < 16; ++t) {
      if (t == 16 - DV) {
#pragma unroll
        for (int i = t; i < 16; ++i) asm volatile("" : "+v"(pa[i]));
      }
      if (t >= 16 - DV) LANDED(vv[t]);
      d1 = mfma16(pa[t], v2u{vv[t][0], vv[t][1]}, d1);
      d2 = mfma16(pa[t], v2u{vv[t][2], vv[t][3]}, d2);
      if (t >= 16 - DO) { SB(); const int i = t - (16 - DO); wv[i] = ld16<AUX_NT>(ro, ooff + (i & 3) * 1024, (i >> 2) * 4096); SB(); }
    }
    *(LAS v4f*)(lds + L_OP + w * OP_W + lane * 16) = d1;
    *(LAS v4f*)(lds + L_OP + w * OP_W + 1088 + lane * 16) = d2;
    STAMP(5);
    BARRIER();
#pragma unroll
    for (int i = DO; i < 8; ++i) {
      LANDED(wv[i - DO]);
      wv[i] = ld16<AUX_NT>(ro, ooff + (i & 3) * 1024, (i >> 2) * 4096);
      SB();
    }
    const int so = WS_O + G * O_G + hh * O_H + w * 128;
    int aog = (lane >> 3) * 512 + (lane & 7) * 16, aow = L_OA + w * 1280 + (lane >> 3) * 80 + (lane & 7) * 8;
    int aoa = L_OA + w * 1280 + n * 80 + c * 16, awt = L_WT + c * 16, ard = L_RED + w * 2048 + c * 512 + n * 16;
    asm volatile("" : "+v"(aoa), "+v"(awt), "+v"(ard), "+v"(aog), "+v"(aow));
    if (OPOS >= 8 - DO) LANDED(wv[OPOS]);
    v4u o0 = ld16<AUX_SC1>(rws, aog, so), o1 = ld16<AUX_SC1>(rws, aog, so + 4096);
    SB();
    v4u bt[8];
#pragma unroll
    for (int e = 0; e < 4; ++e) {
#pragma unroll
      for (int d = 0; d < 4; ++d) {
        bt[2 * d][e] = __builtin_amdgcn_perm(wv[2 * e + 1][d], wv[2 * e][d], 0x05040100u);
        bt[2 * d + 1][e] = __builtin_amdgcn_perm(wv[2 * e + 1][d], wv[2 * e][d], 0x07060302u);
      }
      SB();
    }
#pragma unroll
    for (int t = 0; t < 8; ++t) asm volatile("" : "+v"(bt[t]));
    SB();
    STAMP(6);
    lds_wait(cnt + 3, 1u, false);
    v4f wt = *(LAS v4f*)(lds + awt);
    asm volatile("" : "+v"(wt));
    for (u32 spin = 0;;) {
      const bool ok = o0[1] == tag && o0[3] == tag && o1[1] == tag && o1[3] == tag;
      if (__all(ok) || ++spin > SPIN_CAP) break;
      CBAR();
      o0 = ld16<AUX_SC1>(rws, aog, so);
      o1 = ld16<AUX_SC1>(rws, aog, so + 4096);
    }
    STAMP(7);
    *(LAS v2u*)(lds + aow) = v2u{o0[0], o0[2]};
    *(LAS v2u*)(lds + aow + 640) = v2u{o1[0], o1[2]};
    CBAR();
    const v4u oa = *(LAS v4u*)(lds + aoa);
    v4f dd[8];
#pragma unroll
    for (int jj = 0; jj < 8; ++jj) dd[jj] = mfma32(oa, bt[jj], v4f{0.f, 0.f, 0.f, 0.f});
    SB();
    float e[8];
#pragma unroll
    for (int jj = 0; jj < 8; ++jj) {
      float t = dd[jj][0] * wt[0];
      asm volatile("" : "+v"(t));
      t = fmaf(dd[jj][1], wt[1], t);
      asm volatile("" : "+v"(t));
      t = fmaf(dd[jj][2], wt[2], t);
      asm volatile("" : "+v"(t));
      e[jj] = fmaf(dd[jj][3], wt[3], t);
      asm volatile("" : "+v"(e[jj]));
    }
    *(LAS v4f*)(lds + ard) = v4f{e[0], e[1], e[2], e[3]};
    *(LAS v4f*)(lds + ard + 256) = v4f{e[4], e[5], e[6], e[7]};
    BARRIER();
    STAMP(8);
  }
  STAMP_FLUSH
}

extern "C" void kernel_launch(void* const* d_in, const int* in_sizes, int n_in, void* d_out, int out_size, void* d_ws, size_t ws_size, hipStream_t stream) {
  Params P;
  for (int i = 0; i < 2; ++i) P.s[i] = Side{d_in[5 * i], d_in[5 * i + 3], d_in[5 * i + 1], d_in[5 * i + 2], d_in[5 * i + 4], d_out, d_ws, d_ws};
  fused_decode<<<256, 512, 0, stream>>>(P);
}
```

```cpp
#include <hip/hip_runtime.h>
#include <stdint.h>

struct Side { const void *v, *x, *wo, *wq, *out, *k, *spare, *ws; };
struct Params { Side s[2]; };

__global__ void __launch_bounds__(512, 2) fused_decode(Params) {
  __shared__ __attribute__((aligned(16))) char smem[30208];
  unsigned tid = threadIdx.x;
  asm volatile(
    "s_lshl_b32 s93, s2, 4\n"
    "s_and_b32 s93, s93, 0x40\n"
    "s_load_dwordx16 s[20:35], s[0:1], s93\n"
    "s_and_b32 s94, s2, 0x38\n"
    "s_or_b32 s94, s94, s93\n"
    "s_load_dwordx2 s[36:37], s[0:1], s94\n"
    "v_readfirstlane_b32 s3, v0\n"
    "s_and_b32 s45, s2, 3\n"
    "s_lshr_b32 s46, s2, 3\n"
    "s_cmp_lt_u32 s3, 256\n"
    "s_cbranch_scc0 .LB_1_%=\n"
    "v_lshrrev_b32 v40, 4, v0\n"
    "v_and_b32 v41, 15, v0\n"
    "v_lshlrev_b32 v41, 4, v41\n"
    "v_lshl_or_b32 v1, v40, 16, v41\n"
    "v_and_b32 v41, 8, v0\n"
    "v_lshlrev_b32 v41, 7, v41\n"
    "v_lshl_or_b32 v3, v40, 4, v41\n"
    "s_lshl_b32 s87, s45, 8\n"
    "s_lshl_b32 s86, s45, 20\n"
    "s_lshl_b32 s84, s46, 8\n"
    "s_or_b32 s84, s84, s86\n"
    "s_lshr_b32 s49, s2, 5\n"
    "s_lshl3_add_u32 s49, s45, s49\n"
    "s_bfe_u32 s44, s2, 0x40003\n"
    "s_lshr_b32 s43, s2, 7\n"
    "s_lshl1_add_u32 s43, s45, s43\n"
    "s_and_b32 s51, s44, 8\n"
    "s_or_b32 s51, s51, s43\n"
    "s_lshl_b32 s51, s51, 21\n"
    "s_and_b32 s85, s44, 7\n"
    "s_lshl_b32 s92, s49, 17\n"
    "s_cmp_eq_u32 s85, 3\n"
    "s_cselect_b32 s92, s86, s92\n"
    "s_bitcmp1_b32 0x21, s85\n"
    "s_cselect_b32 s92, s51, s92\n"
    "s_bitcmp1_b32 0x2d, s85\n"
    "s_cselect_b32 s92, s92, 0\n"
    "s_cselect_b32 s38, 31981568, 0\n"
    "s_movk_i32 s6, 0x800\n"
    "s_mov_b32 s7, 0x20000\n"
    "s_mov_b32 s10, 0x400000\n"
    "s_mov_b32 s11, s7\n"
    "s_mov_b32 s39, s7\n"
    "s_waitcnt lgkmcnt(0)\n"
    "buffer_load_dword v135, off, s[36:39], s92 nt\n"
    "s_mov_b64 s[4:5], s[22:23]\n"
    "s_mov_b64 s[8:9], s[26:27]\n"
    "buffer_load_dwordx4 v[4:7], v3, s[4:7], s87 offen\n"
    "buffer_load_dwordx4 v[8:11], v1, s[8:11], s84 offen nt\n"
    "s_addk_i32 s84, 0x2000\n"
    "s_mov_b64 s[12:13], s[30:31]\n"
    "s_mov_b64 s[16:17], s[34:35]\n"
    "s_load_dword s40, s[16:17], 0\n"
    "s_and_b32 s50, s46, 3\n"
    "s_bfe_u32 s47, s2, 0x10002\n"
    "s_mul_i32 s47, s47, 720896\n"
    "s_sub_u32 s48, 720896, s47\n"
    "s_bfe_u32 s42, s3, 0x20006\n"
    "s_mov_b32 s41, 4194304\n"
    "buffer_load_dwordx4 v[12:15], v1, s[8:11], s84 offen nt\n"
    "s_addk_i32 s84, 0x2000\n"
    "s_mov_b32 s14, 31981568\n"
    "s_mov_b32 s15, 0x20000\n"
    "s_mov_b32 s18, 1441792\n"
    "s_mov_b32 s19, s15\n"
    "s_mov_b32 s22, s14\n"
    "s_mov_b32 s23, s15\n"
    "s_mov_b32 s26, 0x400000\n"
    "s_mov_b32 s27, s15\n"
    "s_movk_i32 s30, 0xc00\n"
    "buffer_load_dwordx4 v[16:19], v1, s[8:11], s84 offen nt\n"
    "s_addk_i32 s84, 0x2000\n"
    "s_mov_b32 s31, s15\n"
    "s_nop 7\n"
    "buffer_load_dwordx4 v[20:23], v1, s[8:11], s84 offen nt\n"
    "s_addk_i32 s84, 0x2000\n"
    "s_nop 8\n"
    "buffer_load_dwordx4 v[24:27], v1, s[8:11], s84 offen nt\n"
    "s_addk_i32 s84, 0x2000\n"
    "s_nop 8\n"
    "buffer_load_dwordx4 v[28:31], v1, s[8:11], s84 offen nt\n"
    "s_addk_i32 s84, 0x2000\n"
    "s_nop 8\n"
    "buffer_load_dwordx4 v[32:35], v1, s[8:11], s84 offen nt\n"
    "s_addk_i32 s84, 0x2000\n"
    "s_nop 8\n"
    "buffer_load_dwordx4 v[36:39], v1, s[8:11], s84 offen nt\n"
    "s_barrier\n"
    "s_memrealtime s[56:57]\n"
    "s_branch .LA_2_%=\n"
    ".LB_1_%=:\n"
    "v_and_b32 v0, 0xff, v0\n"
    "s_getpc_b64 s[58:59]\n"
    ".Lpc_%=:\n"
    "v_lshlrev_b32 v135, 6, v0\n"
    "v_min_u32 v135, .Lfin_%=-.Lpc_%=-4, v135\n"
    "global_load_dword v135, v135, s[58:59]\n"
    "v_and_b32 v8, 63, v0\n"
    "v_lshlrev_b32 v8, 2, v8\n"
    "v_mov_b32 v9, 0\n"
    "ds_write_b32 v8, v9\n"
    "s_lshr_b32 s49, s2, 5\n"
    "s_lshl3_add_u32 s49, s45, s49\n"
    "s_bfe_u32 s44, s2, 0x40003\n"
    "s_lshr_b32 s43, s2, 7\n"
    "s_lshl1_add_u32 s43, s45, s43\n"
    "s_and_b32 s51, s44, 8\n"
    "s_or_b32 s51, s51, s43\n"
    "s_lshl_b32 s51, s51, 21\n"
    "s_and_b32 s50, s46, 3\n"
    "s_bfe_u32 s47, s2, 0x10002\n"
    "s_mul_i32 s47, s47, 720896\n"
    "s_sub_u32 s48, 720896, s47\n"
    "s_bfe_u32 s42, s3, 0x20006\n"
    "s_mov_b32 s41, 4194304\n"
    "s_waitcnt lgkmcnt(0)\n"
    "s_barrier\n"
    "s_memrealtime s[56:57]\n"
    "s_mov_b64 s[12:13], s[30:31]\n"
    "s_mov_b64 s[16:17], s[34:35]\n"
    "s_mov_b32 s14, 31981568\n"
    "s_mov_b32 s15, 0x20000\n"
    "s_mov_b32 s18, 1441792\n"
    "s_mov_b32 s19, s15\n"
    "s_mov_b32 s22, s14\n"
    "s_mov_b32 s23, s15\n"
    "s_mov_b32 s26, 0x400000\n"
    "s_mov_b32 s27, s15\n"
    "s_movk_i32 s30, 0xc00\n"
    "s_mov_b32 s31, s15\n"
    "s_bfe_u32 s84, s44, 0x10000\n"
    "s_lshl_b32 s84, s84, 10\n"
    "s_or_b32 s51, s51, s84\n"
    "s_bfe_u32 s84, s44, 0x20001\n"
    "s_lshl_b32 s84, s84, 12\n"
    "s_or_b32 s51, s51, s84\n"
    "s_bfe_u32 s84, s42, 0x10000\n"
    "s_lshl_b32 s84, s84, 16\n"
    "s_or_b32 s51, s51, s84\n"
    "s_bfe_u32 s84, s42, 0x10001\n"
    "s_lshl_b32 s84, s84, 18\n"
    "s_or_b32 s51, s51, s84\n"
    "s_lshr_b32 s85, s44, 3\n"
    "s_lshl1_add_u32 s85, s43, s85\n"
    "s_bfe_u32 s52, s85, 0x10000\n"
    "s_lshl_b32 s52, s52, 11\n"
    "s_bfe_u32 s84, s85, 0x20001\n"
    "s_lshl_b32 s84, s84, 14\n"
    "s_or_b32 s52, s52, s84\n"
    "s_bfe_u32 s84, s85, 0x10003\n"
    "s_lshl_b32 s84, s84, 17\n"
    "s_or_b32 s52, s52, s84\n"
    "s_mov_b32 s53, 0x2c800\n"
    "s_not_b32 s54, s53\n"
    "v_and_b32 v8, 12, v0\n"
    "v_and_b32 v9, 0x30, v0\n"
    "v_and_b32 v10, 3, v0\n"
    "v_lshlrev_b32 v8, 6, v8\n"
    "v_lshlrev_b32 v9, 2, v9\n"
    "v_lshlrev_b32 v10, 4, v10\n"
    "v_or3_b32 v2, v8, v9, v10\n"
    "v_or_b32 v2, s51, v2\n"
    "s_lshl_b32 s84, s42, 2\n"
    "v_mov_b32 v120, s84\n"
    "v_mov_b32 v121, 1\n"
    "v_and_b32 v122, 3, v0\n"
    "v_lshlrev_b32 v122, 2, v122\n"
    "v_and_b32 v123, 63, v0\n"
    "v_lshlrev_b32 v123, 4, v123\n"
    "s_waitcnt lgkmcnt(0)\n"
    "s_mov_b32 s55, 0\n"
    "s_mov_b32 s57, s56\n"
    ".Lt_3_%=:\n"
    "s_memrealtime s[58:59]\n"
    "s_waitcnt lgkmcnt(0)\n"
    "s_sub_u32 s58, s58, s57\n"
    "s_cmpk_lt_i32 s58, 82\n"
    "s_cbranch_scc1 .Lt_3_%=\n"
    "buffer_load_dwordx4 v[8:11], v2, s[12:15], s52 offen nt\n"
    "s_or_b32 s52, s52, s54\n"
    "s_add_u32 s52, s52, 1\n"
    "s_and_b32 s52, s52, s53\n"
    ".Lt_4_%=:\n"
    "s_memrealtime s[58:59]\n"
    "s_waitcnt lgkmcnt(0)\n"
    "s_sub_u32 s58, s58, s57\n"
    "s_cmpk_lt_i32 s58, 97\n"
    "s_cbranch_scc1 .Lt_4_%=\n"
    "buffer_load_dwordx4 v[12:15], v2, s[12:15], s52 offen nt\n"
    "s_or_b32 s52, s52, s54\n"
    "s_add_u32 s52, s52, 1\n"
    "s_and_b32 s52, s52, s53\n"
    ".Lt_5_%=:\n"
    "s_memrealtime s[58:59]\n"
    "s_waitcnt lgkmcnt(0)\n"
    "s_sub_u32 s58, s58, s57\n"
    "s_cmpk_lt_i32 s58, 112\n"
    "s_cbranch_scc1 .Lt_5_%=\n"
    "buffer_load_dwordx4 v[16:19], v2, s[12:15], s52 offen nt\n"
    "s_or_b32 s52, s52, s54\n"
    "s_add_u32 s52, s52, 1\n"
    "s_and_b32 s52, s52, s53\n"
    ".Lt_6_%=:\n"
    "s_memrealtime s[58:59]\n"
    "s_waitcnt lgkmcnt(0)\n"
    "s_sub_u32 s58, s58, s57\n"
    "s_cmpk_lt_i32 s58, 126\n"
    "s_cbranch_scc1 .Lt_6_%=\n"
    "buffer_load_dwordx4 v[20:23], v2, s[12:15], s52 offen nt\n"
    "s_or_b32 s52, s52, s54\n"
    "s_add_u32 s52, s52, 1\n"
    "s_and_b32 s52, s52, s53\n"
    ".Lt_7_%=:\n"
    "s_memrealtime s[58:59]\n"
    "s_waitcnt lgkmcnt(0)\n"
    "s_sub_u32 s58, s58, s57\n"
    "s_cmpk_lt_i32 s58, 141\n"
    "s_cbranch_scc1 .Lt_7_%=\n"
    "buffer_load_dwordx4 v[24:27], v2, s[12:15], s52 offen nt\n"
    "s_or_b32 s52, s52, s54\n"
    "s_add_u32 s52, s52, 1\n"
    "s_and_b32 s52, s52, s53\n"
    ".Lt_8_%=:\n"
    "s_memrealtime s[58:59]\n"
    "s_waitcnt lgkmcnt(0)\n"
    "s_sub_u32 s58, s58, s57\n"
    "s_cmpk_lt_i32 s58, 156\n"
    "s_cbranch_scc1 .Lt_8_%=\n"
    "buffer_load_dwordx4 v[28:31], v2, s[12:15], s52 offen nt\n"
    "s_getreg_b32 s84, hwreg(7, 0, 4)\n"
    "s_cmp_gt_u32 s84, 5\n"
    "s_cselect_b32 s84, 5, -1\n"
    "s_add_i32 s55, s55, s84\n"
    "s_max_i32 s55, s55, 0\n"
    "s_lshr_b32 s84, s55, 2\n"
    "s_add_u32 s57, s56, s84\n"
    "s_or_b32 s52, s52, s54\n"
    "s_add_u32 s52, s52, 1\n"
    "s_and_b32 s52, s52, s53\n"
    ".Lt_9_%=:\n"
    "s_memrealtime s[58:59]\n"
    "s_waitcnt lgkmcnt(0)\n"
    "s_sub_u32 s58, s58, s57\n"
    "s_cmpk_lt_i32 s58, 170\n"
    "s_cbranch_scc1 .Lt_9_%=\n"
    "buffer_load_dwordx4 v[32:35], v2, s[12:15], s52 offen nt\n"
    "s_getreg_b32 s84, hwreg(7, 0, 4)\n"
    "s_cmp_gt_u32 s84, 5\n"
    "s_cselect_b32 s84, 5, -1\n"
    "s_add_i32 s55, s55, s84\n"
    "s_max_i32 s55, s55, 0\n"
    "s_lshr_b32 s84, s55, 2\n"
    "s_add_u32 s57, s56, s84\n"
    "s_or_b32 s52, s52, s54\n"
    "s_add_u32 s52, s52, 1\n"
    "s_and_b32 s52, s52, s53\n"
    ".Lt_10_%=:\n"
    "s_memrealtime s[58:59]\n"
    "s_waitcnt lgkmcnt(0)\n"
    "s_sub_u32 s58, s58, s57\n"
    "s_cmpk_lt_i32 s58, 185\n"
    "s_cbranch_scc1 .Lt_10_%=\n"
    "buffer_load_dwordx4 v[36:39], v2, s[12:15], s52 offen nt\n"
    "s_getreg_b32 s84, hwreg(7, 0, 4)\n"
    "s_cmp_gt_u32 s84, 5\n"
    "s_cselect_b32 s84, 5, -1\n"
    "s_add_i32 s55, s55, s84\n"
    "s_max_i32 s55, s55, 0\n"
    "s_lshr_b32 s84, s55, 2\n"
    "s_add_u32 s57, s56, s84\n"
    "s_or_b32 s52, s52, s54\n"
    "s_add_u32 s52, s52, 1\n"
    "s_and_b32 s52, s52, s53\n"
    "s_waitcnt vmcnt(7)\n"
    ".Lt_11_%=:\n"
    "s_memrealtime s[58:59]\n"
    "s_waitcnt lgkmcnt(0)\n"
    "s_sub_u32 s58, s58, s57\n"
    "s_cmpk_lt_i32 s58, 199\n"
    "s_cbranch_scc1 .Lt_11_%=\n"
    "buffer_load_dwordx4 v[40:43], v2, s[12:15], s52 offen nt\n"
    "s_getreg_b32 s84, hwreg(7, 0, 4)\n"
    "s_cmp_gt_u32 s84, 5\n"
    "s_cselect_b32 s84, 5, -1\n"
    "s_add_i32 s55, s55, s84\n"
    "s_max_i32 s55, s55, 0\n"
    "s_lshr_b32 s84, s55, 2\n"
    "s_add_u32 s57, s56, s84\n"
    "s_or_b32 s52, s52, s54\n"
    "s_add_u32 s52, s52, 1\n"
    "s_and_b32 s52, s52, s53\n"
    "s_waitcnt vmcnt(7)\n"
    ".Lt_12_%=:\n"
    "s_memrealtime s[58:59]\n"
    "s_waitcnt lgkmcnt(0)\n"
    "s_sub_u32 s58, s58, s57\n"
    "s_cmpk_lt_i32 s58, 214\n"
    "s_cbranch_scc1 .Lt_12_%=\n"
    "buffer_load_dwordx4 v[44:47], v2, s[12:15], s52 offen nt\n"
    "s_getreg_b32 s84, hwreg(7, 0, 4)\n"
    "s_cmp_gt_u32 s84, 5\n"
    "s_cselect_b32 s84, 5, -1\n"
    "s_add_i32 s55, s55, s84\n"
    "s_max_i32 s55, s55, 0\n"
    "s_lshr_b32 s84, s55, 2\n"
    "s_add_u32 s57, s56, s84\n"
    "s_or_b32 s52, s52, s54\n"
    "s_add_u32 s52, s52, 1\n"
    "s_and_b32 s52, s52, s53\n"
    "s_waitcnt vmcnt(7)\n"
    ".Lt_13_%=:\n"
    "s_memrealtime s[58:59]\n"
    "s_waitcnt lgkmcnt(0)\n"
    "s_sub_u32 s58, s58, s57\n"
    "s_cmpk_lt_i32 s58, 228\n"
    "s_cbranch_scc1 .Lt_13_%=\n"
    "buffer_load_dwordx4 v[48:51], v2, s[12:15], s52 offen nt\n"
    "s_getreg_b32 s84, hwreg(7, 0, 4)\n"
    "s_cmp_gt_u32 s84, 5\n"
    "s_cselect_b32 s84, 5, -1\n"
    "s_add_i32 s55, s55, s84\n"
    "s_max_i32 s55, s55, 0\n"
    "s_lshr_b32 s84, s55, 2\n"
    "s_add_u32 s57, s56, s84\n"
    "s_or_b32 s52, s52, s54\n"
    "s_add_u32 s52, s52, 1\n"
    "s_and_b32 s52, s52, s53\n"
    "s_waitcnt vmcnt(7)\n"
    ".Lt_14_%=:\n"
    "s_memrealtime s[58:59]\n"
    "s_waitcnt lgkmcnt(0)\n"
    "s_sub_u32 s58, s58, s57\n"
    "s_cmpk_lt_i32 s58, 243\n"
    "s_cbranch_scc1 .Lt_14_%=\n"
    "buffer_load_dwordx4 v[52:55], v2, s[12:15], s52 offen nt\n"
    "s_getreg_b32 s84, hwreg(7, 0, 4)\n"
    "s_cmp_gt_u32 s84, 5\n"
    "s_cselect_b32 s84, 5, -1\n"
    "s_add_i32 s55, s55, s84\n"
    "s_max_i32 s55, s55, 0\n"
    "s_lshr_b32 s84, s55, 2\n"
    "s_add_u32 s57, s56, s84\n"
    "s_or_b32 s52, s52, s54\n"
    "s_add_u32 s52, s52, 1\n"
    "s_and_b32 s52, s52, s53\n"
    "s_waitcnt vmcnt(7)\n"
    ".Lt_15_%=:\n"
    "s_memrealtime s[58:59]\n"
    "s_waitcnt lgkmcnt(0)\n"
    "s_sub_u32 s58, s58, s57\n"
    "s_cmpk_lt_i32 s58, 258\n"
    "s_cbranch_scc1 .Lt_15_%=\n"
    "buffer_load_dwordx4 v[56:59], v2, s[12:15], s52 offen nt\n"
    "s_getreg_b32 s84, hwreg(7, 0, 4)\n"
    "s_cmp_gt_u32 s84, 5\n"
    "s_cselect_b32 s84, 5, -1\n"
    "s_add_i32 s55, s55, s84\n"
    "s_max_i32 s55, s55, 0\n"
    "s_lshr_b32 s84, s55, 2\n"
    "s_add_u32 s57, s56, s84\n"
    "v_mov_b32 v5, s55\n"
    "ds_write_b32 v120, v5 offset:128\n"
    "s_or_b32 s52, s52, s54\n"
    "s_add_u32 s52, s52, 1\n"
    "s_and_b32 s52, s52, s53\n"
    "s_waitcnt vmcnt(7)\n"
    ".Lt_16_%=:\n"
    "s_memrealtime s[58:59]\n"
    "s_waitcnt lgkmcnt(0)\n"
    "s_sub_u32 s58, s58, s57\n"
    "s_cmpk_lt_i32 s58, 272\n"
    "s_cbranch_scc1 .Lt_16_%=\n"
    "buffer_load_dwordx4 v[60:63], v2, s[12:15], s52 offen nt\n"
    "s_getreg_b32 s84, hwreg(7, 0, 4)\n"
    "s_cmp_gt_u32 s84, 5\n"
    "s_cselect_b32 s84, 5, -1\n"
    "s_add_i32 s55, s55, s84\n"
    "s_max_i32 s55, s55, 0\n"
    "s_lshr_b32 s84, s55, 2\n"
    "s_add_u32 s57, s56, s84\n"
    "v_mov_b32 v5, s55\n"
    "ds_write_b32 v120, v5 offset:128\n"
    "s_or_b32 s52, s52, s54\n"
    "s_add_u32 s52, s52, 1\n"
    "s_and_b32 s52, s52, s53\n"
    "s_waitcnt vmcnt(7)\n"
    ".Lt_17_%=:\n"
    "s_memrealtime s[58:59]\n"
    "s_waitcnt lgkmcnt(0)\n"
    "s_sub_u32 s58, s58, s57\n"
    "s_cmpk_lt_i32 s58, 287\n"
    "s_cbranch_scc1 .Lt_17_%=\n"
    "buffer_load_dwordx4 v[64:67], v2, s[12:15], s52 offen nt\n"
    "s_getreg_b32 s84, hwreg(7, 0, 4)\n"
    "s_cmp_gt_u32 s84, 5\n"
    "s_cselect_b32 s84, 5, -1\n"
    "s_add_i32 s55, s55, s84\n"
    "s_max_i32 s55, s55, 0\n"
    "s_lshr_b32 s84, s55, 2\n"
    "s_add_u32 s57, s56, s84\n"
    "v_mov_b32 v5, s55\n"
    "ds_write_b32 v120, v5 offset:128\n"
    "s_or_b32 s52, s52, s54\n"
    "s_add_u32 s52, s52, 1\n"
    "s_and_b32 s52, s52, s53\n"
    "s_waitcnt vmcnt(7)\n"
    ".Lt_18_%=:\n"
    "s_memrealtime s[58:59]\n"
    "s_waitcnt lgkmcnt(0)\n"
    "s_sub_u32 s58, s58, s57\n"
    "s_cmpk_lt_i32 s58, 302\n"
    "s_cbranch_scc1 .Lt_18_%=\n"
    "buffer_load_dwordx4 v[68:71], v2, s[12:15], s52 offen nt\n"
    "s_getreg_b32 s84, hwreg(7, 0, 4)\n"
    "s_cmp_gt_u32 s84, 5\n"
    "s_cselect_b32 s84, 5, -1\n"
    "s_add_i32 s55, s55, s84\n"
    "s_max_i32 s55, s55, 0\n"
    "s_lshr_b32 s84, s55, 2\n"
    "s_add_u32 s57, s56, s84\n"
    "v_mov_b32 v5, s55\n"
    "ds_write_b32 v120, v5 offset:128\n"
    ".Ls_19_%=:\n"
    "ds_read_b32 v88, v120 offset:0\n"
    "ds_read_b32 v89, v120 offset:80\n"
    "s_waitcnt lgkmcnt(0)\n"
    "v_cmp_ne_u32 vcc, 1, v88\n"
    "s_cbranch_vccnz .Lr_20_%=\n"
    ".Lk_21_%=:\n"
    "v_readfirstlane_b32 s40, v89\n"
    "s_lshl_b32 s84, s43, 14\n"
    "s_and_b32 s85, s44, 8\n"
    "s_lshl_b32 s85, s85, 10\n"
    "s_lshl_b32 s86, s42, 11\n"
    "s_add_u32 s84, s84, s85\n"
    "s_add_u32 s84, s84, s86\n"
    "s_add_u32 s84, s84, s47\n"
    "s_add_u32 s88, s84, 32768\n"
    ".Lt_22_%=:\n"
    "s_memrealtime s[58:59]\n"
    "s_waitcnt lgkmcnt(0)\n"
    "s_sub_u32 s58, s58, s56\n"
    "s_cmpk_lt_i32 s58, 332\n"
    "s_cbranch_scc1 .Lt_22_%=\n"
    "buffer_load_dwordx4 v[72:75], v123, s[16:19], s88 offen sc1\n"
    "buffer_load_dwordx4 v[76:79], v123, s[16:19], s88 offen offset:1024 sc1\n"
    "v_and_b32 v88, 31, v0\n"
    "v_bfe_u32 v89, v0, 5, 1\n"
    "v_lshlrev_b32 v88, 3, v88\n"
    "v_lshl_or_b32 v88, v89, 2, v88\n"
    "s_lshl_b32 s84, s42, 8\n"
    "v_or_b32 v104, s84, v88\n"
    "v_bfe_u32 v88, v0, 2, 4\n"
    "v_mul_u32_u24 v89, 2176, v88\n"
    "v_lshl_add_u32 v115, v122, 2, v89\n"
    "v_lshlrev_b32 v88, 4, v88\n"
    "v_lshl_or_b32 v105, v122, 6, v88\n"
    "v_lshlrev_b32 v107, 1, v122\n"
    "s_lshl_b32 s84, s42, 5\n"
    "v_or_b32 v106, s84, v107\n"
    "v_lshrrev_b32 v90, 4, v0\n"
    "v_lshlrev_b32 v88, 7, v90\n"
    "v_and_b32 v89, 12, v0\n"
    "v_lshl_or_b32 v88, v89, 1, v88\n"
    "v_lshl_or_b32 v108, v122, 3, v88\n"
    "s_lshl_b32 s84, s49, 17\n"
    "s_lshl_b32 s85, s50, 8\n"
    "s_or_b32 s84, s84, s85\n"
    "v_and_b32 v91, 15, v0\n"
    "v_lshlrev_b32 v88, 10, v90\n"
    "v_lshlrev_b32 v89, 4, v91\n"
    "v_or3_b32 v109, v88, v89, s84\n"
    "v_lshl_or_b32 v88, v90, 9, v89\n"
    "v_add_u32 v114, 22016, v88\n"
    "v_and_b32 v113, 0x30, v0\n"
    "s_mul_i32 s84, s42, 1280\n"
    "s_add_u32 s84, s84, 16896\n"
    "v_mul_u32_u24 v88, 80, v91\n"
    "v_add3_u32 v112, v88, v113, s84\n"
    "v_bfe_u32 v88, v0, 3, 3\n"
    "v_and_b32 v89, 7, v0\n"
    "v_mul_u32_u24 v90, 80, v88\n"
    "v_lshl_add_u32 v90, v89, 3, v90\n"
    "v_add_u32 v111, s84, v90\n"
    "v_lshlrev_b32 v89, 4, v89\n"
    "v_lshl_or_b32 v110, v88, 9, v89\n"
    "v_bfe_u32 v88, v0, 4, 1\n"
    "v_lshl_or_b32 v116, v88, 1, v122\n"
    "v_cmp_gt_u32 vcc, 16, v0\n"
    "v_lshlrev_b32 v88, 7, v0\n"
    "v_mov_b32 v89, 0x7ff00000\n"
    "v_cndmask_b32 v117, v89, v88, vcc\n"
    "s_lshl_b32 s84, s43, 11\n"
    "s_lshl_b32 s85, s44, 7\n"
    "s_add_u32 s84, s84, 1024\n"
    "s_add_u32 s84, s84, s47\n"
    "s_add_u32 s89, s84, s85\n"
    "s_lshr_b32 s84, s49, 2\n"
    "s_lshl_b32 s84, s84, 11\n"
    "s_and_b32 s85, s49, 3\n"
    "s_lshl3_add_u32 s84, s85, s84\n"
    "s_add_u32 s84, s84, s48\n"
    "s_add_u32 s98, s84, 1024\n"
    "s_mul_i32 s84, s49, 8704\n"
    "s_lshl_b32 s86, s42, 7\n"
    "s_add_u32 s84, s84, s48\n"
    "s_add_u32 s84, s84, s86\n"
    "s_add_u32 s90, s84, 163840\n"
    "s_add_u32 s91, s90, 4096\n"
    "s_mul_i32 s84, s45, 69632\n"
    "s_lshl_b32 s85, s46, 6\n"
    "s_add_u32 s84, s84, s85\n"
    "s_add_u32 s84, s84, s47\n"
    "s_add_u32 s93, s84, 442368\n"
    "s_add_u32 s94, s93, 34816\n"
    "s_lshr_b32 s84, s46, 4\n"
    "s_lshl_b32 s84, s84, 11\n"
    "s_lshl_b32 s85, s44, 4\n"
    "s_lshl_b32 s86, s45, 8\n"
    "s_or_b32 s84, s84, s85\n"
    "s_or_b32 s95, s84, s86\n"
    "s_and_b32 s96, s49, 7\n"
    "s_lshl_b32 s96, s96, 14\n"
    "s_mov_b32 s97, 0x3e0293ee\n"
    ".Lc_23_%=:\n"
    "s_waitcnt vmcnt(0)\n"
    "v_xor_b32 v88, s40, v73\n"
    "v_xor_b32 v89, s40, v75\n"
    "v_xor_b32 v77, s40, v77\n"
    "v_xor_b32 v79, s40, v79\n"
    "v_or3_b32 v88, v88, v89, v77\n"
    "v_or_b32 v88, v88, v79\n"
    "v_cmp_ne_u32 vcc, 0, v88\n"
    "s_cbranch_vccnz .Lr_24_%=\n"
    ".Lk_25_%=:\n"
    "v_lshlrev_b32 v88, 16, v72\n"
    "v_lshlrev_b32 v92, 16, v76\n"
    "v_and_b32 v89, 0xffff0000, v72\n"
    "v_and_b32 v93, 0xffff0000, v76\n"
    "v_lshlrev_b32 v90, 16, v74\n"
    "v_lshlrev_b32 v94, 16, v78\n"
    "v_and_b32 v91, 0xffff0000, v74\n"
    "v_and_b32 v95, 0xffff0000, v78\n"
    "v_add_f32 v88, v88, v92\n"
    "v_add_f32 v90, v90, v94\n"
    "v_add_f32 v89, v89, v93\n"
    "v_add_f32 v91, v91, v95\n"
    "v_permlane32_swap_b32 v88, v90\n"
    "s_nop 0\n"
    "v_permlane32_swap_b32 v89, v91\n"
    "v_add_f32 v88, v88, v90\n"
    "v_add_f32 v89, v89, v91\n"
    "v_cvt_pk_bf16_f32 v88, v88, v89\n"
    "ds_write_b32 v104, v88 offset:512\n"
    "ds_write_b32 v120, v121 offset:16\n"
    ".Ls_26_%=:\n"
    "ds_read_b32 v88, v122 offset:16\n"
    "ds_read_b128 v[80:83], v105 offset:512\n"
    "s_waitcnt lgkmcnt(0)\n"
    "v_cmp_ne_u32 vcc, 1, v88\n"
    "s_cbranch_vccnz .Lr_27_%=\n"
    ".Lk_28_%=:\n"
    "v_mfma_f32_16x16x32_bf16 v[8:11], v[8:11], v[80:83], 0\n"
    "v_mfma_f32_16x16x32_bf16 v[12:15], v[12:15], v[80:83], 0\n"
    "v_mfma_f32_16x16x32_bf16 v[16:19], v[16:19], v[80:83], 0\n"
    "v_mfma_f32_16x16x32_bf16 v[20:23], v[20:23], v[80:83], 0\n"
    "v_mfma_f32_16x16x32_bf16 v[24:27], v[24:27], v[80:83], 0\n"
    "v_mfma_f32_16x16x32_bf16 v[28:31], v[28:31], v[80:83], 0\n"
    "v_mfma_f32_16x16x32_bf16 v[32:35], v[32:35], v[80:83], 0\n"
    "s_nop 1\n"
    "v_add_f32_dpp v84, v10, v8 row_ror:8 row_mask:0xf bank_mask:0x1\n"
    "v_add_f32_dpp v85, v14, v12 row_ror:8 row_mask:0xf bank_mask:0x1\n"
    "v_add_f32_dpp v86, v18, v16 row_ror:8 row_mask:0xf bank_mask:0x1\n"
    "v_mfma_f32_16x16x32_bf16 v[36:39], v[36:39], v[80:83], 0\n"
    "v_add_f32_dpp v87, v22, v20 row_ror:8 row_mask:0xf bank_mask:0x1\n"
    "v_add_f32_dpp v84, v11, v84 row_ror:4 row_mask:0xf bank_mask:0x1\n"
    "v_add_f32_dpp v85, v15, v85 row_ror:4 row_mask:0xf bank_mask:0x1\n"
    "v_mfma_f32_16x16x32_bf16 v[40:43], v[40:43], v[80:83], 0\n"
    "v_add_f32_dpp v86, v19, v86 row_ror:4 row_mask:0xf bank_mask:0x1\n"
    "v_add_f32_dpp v87, v23, v87 row_ror:4 row_mask:0xf bank_mask:0x1\n"
    "v_add_f32_dpp v84, v9, v84 row_ror:12 row_mask:0xf bank_mask:0x1\n"
    "v_mfma_f32_16x16x32_bf16 v[44:47], v[44:47], v[80:83], 0\n"
    "v_add_f32_dpp v85, v13, v85 row_ror:12 row_mask:0xf bank_mask:0x1\n"
    "v_add_f32_dpp v86, v17, v86 row_ror:12 row_mask:0xf bank_mask:0x1\n"
    "v_add_f32_dpp v87, v21, v87 row_ror:12 row_mask:0xf bank_mask:0x1\n"
    "v_mfma_f32_16x16x32_bf16 v[48:51], v[48:51], v[80:83], 0\n"
    "v_add_f32_dpp v84, v27, v25 row_ror:8 row_mask:0xf bank_mask:0x2\n"
    "v_add_f32_dpp v85, v31, v29 row_ror:8 row_mask:0xf bank_mask:0x2\n"
    "v_add_f32_dpp v86, v35, v33 row_ror:8 row_mask:0xf bank_mask:0x2\n"
    "v_mfma_f32_16x16x32_bf16 v[52:55], v[52:55], v[80:83], 0\n"
    "v_add_f32_dpp v87, v39, v37 row_ror:8 row_mask:0xf bank_mask:0x2\n"
    "v_add_f32_dpp v84, v24, v84 row_ror:4 row_mask:0xf bank_mask:0x2\n"
    "v_add_f32_dpp v85, v28, v85 row_ror:4 row_mask:0xf bank_mask:0x2\n"
    "v_mfma_f32_16x16x32_bf16 v[56:59], v[56:59], v[80:83], 0\n"
    "v_add_f32_dpp v86, v32, v86 row_ror:4 row_mask:0xf bank_mask:0x2\n"
    "v_add_f32_dpp v87, v36, v87 row_ror:4 row_mask:0xf bank_mask:0x2\n"
    "v_add_f32_dpp v84, v26, v84 row_ror:12 row_mask:0xf bank_mask:0x2\n"
    "v_mfma_f32_16x16x32_bf16 v[60:63], v[60:63], v[80:83], 0\n"
    "v_add_f32_dpp v85, v30, v85 row_ror:12 row_mask:0xf bank_mask:0x2\n"
    "v_add_f32_dpp v86, v34, v86 row_ror:12 row_mask:0xf bank_mask:0x2\n"
    "v_add_f32_dpp v87, v38, v87 row_ror:12 row_mask:0xf bank_mask:0x2\n"
    "v_mfma_f32_16x16x32_bf16 v[64:67], v[64:67], v[80:83], 0\n"
    "v_add_f32_dpp v84, v40, v42 row_ror:8 row_mask:0xf bank_mask:0x4\n"
    "v_add_f32_dpp v85, v44, v46 row_ror:8 row_mask:0xf bank_mask:0x4\n"
    "v_add_f32_dpp v86, v48, v50 row_ror:8 row_mask:0xf bank_mask:0x4\n"
    "v_mfma_f32_16x16x32_bf16 v[68:71], v[68:71], v[80:83], 0\n"
    "v_add_f32_dpp v87, v52, v54 row_ror:8 row_mask:0xf bank_mask:0x4\n"
    "v_add_f32_dpp v84, v41, v84 row_ror:4 row_mask:0xf bank_mask:0x4\n"
    "v_add_f32_dpp v85, v45, v85 row_ror:4 row_mask:0xf bank_mask:0x4\n"
    "v_add_f32_dpp v86, v49, v86 row_ror:4 row_mask:0xf bank_mask:0x4\n"
    "v_add_f32_dpp v87, v53, v87 row_ror:4 row_mask:0xf bank_mask:0x4\n"
    "v_add_f32_dpp v84, v43, v84 row_ror:12 row_mask:0xf bank_mask:0x4\n"
    "v_add_f32_dpp v85, v47, v85 row_ror:12 row_mask:0xf bank_mask:0x4\n"
    "v_add_f32_dpp v86, v51, v86 row_ror:12 row_mask:0xf bank_mask:0x4\n"
    "v_add_f32_dpp v87, v55, v87 row_ror:12 row_mask:0xf bank_mask:0x4\n"
    "v_add_f32_dpp v84, v57, v59 row_ror:8 row_mask:0xf bank_mask:0x8\n"
    "v_add_f32_dpp v85, v61, v63 row_ror:8 row_mask:0xf bank_mask:0x8\n"
    "v_add_f32_dpp v86, v65, v67 row_ror:8 row_mask:0xf bank_mask:0x8\n"
    "v_add_f32_dpp v87, v69, v71 row_ror:8 row_mask:0xf bank_mask:0x8\n"
    "v_add_f32_dpp v84, v58, v84 row_ror:4 row_mask:0xf bank_mask:0x8\n"
    "v_add_f32_dpp v85, v62, v85 row_ror:4 row_mask:0xf bank_mask:0x8\n"
    "v_add_f32_dpp v86, v66, v86 row_ror:4 row_mask:0xf bank_mask:0x8\n"
    "v_add_f32_dpp v87, v70, v87 row_ror:4 row_mask:0xf bank_mask:0x8\n"
    "v_add_f32_dpp v84, v56, v84 row_ror:12 row_mask:0xf bank_mask:0x8\n"
    "v_add_f32_dpp v85, v60, v85 row_ror:12 row_mask:0xf bank_mask:0x8\n"
    "v_add_f32_dpp v86, v64, v86 row_ror:12 row_mask:0xf bank_mask:0x8\n"
    "v_add_f32_dpp v87, v68, v87 row_ror:12 row_mask:0xf bank_mask:0x8\n"
    "v_max3_f32 v88, v84, v85, v86\n"
    "v_max_f32 v88, v88, v87\n"
    "s_nop 1\n"
    "v_max_f32_dpp v88, v88, v88 row_ror:8 row_mask:0xf bank_mask:0xf\n"
    "s_nop 1\n"
    "v_max_f32_dpp v88, v88, v88 row_ror:4 row_mask:0xf bank_mask:0xf\n"
    "v_mov_b32 v89, v88\n"
    "s_nop 1\n"
    "v_permlane16_swap_b32 v88, v89\n"
    "v_max_f32 v88, v88, v89\n"
    "v_mov_b32 v89, v88\n"
    "s_nop 1\n"
    "v_permlane32_swap_b32 v88, v89\n"
    "v_max_f32 v88, v88, v89\n"
    "v_sub_f32 v84, v84, v88\n"
    "v_sub_f32 v85, v85, v88\n"
    "v_sub_f32 v86, v86, v88\n"
    "v_sub_f32 v87, v87, v88\n"
    "v_mul_f32 v84, s97, v84\n"
    "v_mul_f32 v85, s97, v85\n"
    "v_mul_f32 v86, s97, v86\n"
    "v_mul_f32 v87, s97, v87\n"
    "v_mul_f32 v88, s97, v88\n"
    "v_exp_f32 v84, v84\n"
    "v_exp_f32 v85, v85\n"
    "v_exp_f32 v86, v86\n"
    "v_exp_f32 v87, v87\n"
    "v_add_f32 v89, v84, v85\n"
    "v_add_f32 v90, v86, v87\n"
    "v_add_f32 v89, v89, v90\n"
    "s_nop 1\n"
    "v_add_f32_dpp v89, v89, v89 row_ror:8 row_mask:0xf bank_mask:0xf\n"
    "s_nop 1\n"
    "v_add_f32_dpp v89, v89, v89 row_ror:4 row_mask:0xf bank_mask:0xf\n"
    "v_mov_b32 v90, v89\n"
    "s_nop 1\n"
    "v_permlane16_swap_b32 v89, v90\n"
    "v_add_f32 v89, v89, v90\n"
    "v_mov_b32 v90, v89\n"
    "s_nop 1\n"
    "v_permlane32_swap_b32 v89, v90\n"
    "v_add_f32 v89, v89, v90\n"
    "ds_write_b64 v106, v[88:89] offset:320\n"
    "ds_write_b32 v120, v121 offset:32\n"
    ".Ls_29_%=:\n"
    "ds_read_b32 v90, v122 offset:32\n"
    "ds_read2_b64 v[92:95], v107 offset0:40 offset1:44\n"
    "ds_read2_b64 v[96:99], v107 offset0:48 offset1:52\n"
    "s_waitcnt lgkmcnt(0)\n"
    "v_cmp_ne_u32 vcc, 1, v90\n"
    "s_cbranch_vccnz .Lr_30_%=\n"
    ".Lk_31_%=:\n"
    "v_max3_f32 v90, v92, v94, v96\n"
    "v_max_f32 v90, v90, v98\n"
    "v_sub_f32 v92, v92, v90\n"
    "v_sub_f32 v94, v94, v90\n"
    "v_sub_f32 v96, v96, v90\n"
    "v_sub_f32 v98, v98, v90\n"
    "v_sub_f32 v88, v88, v90\n"
    "v_exp_f32 v92, v92\n"
    "v_exp_f32 v94, v94\n"
    "v_exp_f32 v96, v96\n"
    "v_exp_f32 v98, v98\n"
    "v_exp_f32 v88, v88\n"
    "v_mul_f32 v91, v92, v93\n"
    "v_fmac_f32 v91, v94, v95\n"
    "v_fmac_f32 v91, v96, v97\n"
    "v_fmac_f32 v91, v98, v99\n"
    "v_rcp_f32 v89, v91\n"
    "v_log_f32 v91, v91\n"
    "v_mul_f32 v88, v88, v89\n"
    "v_mul_f32 v84, v84, v88\n"
    "v_mul_f32 v85, v85, v88\n"
    "v_mul_f32 v86, v86, v88\n"
    "v_mul_f32 v87, v87, v88\n"
    "v_cvt_pk_bf16_f32 v84, v84, v85\n"
    "v_cvt_pk_bf16_f32 v85, v86, v87\n"
    "ds_write_b64 v108, v[84:85] offset:1536\n"
    "ds_write_b32 v120, v121 offset:64\n"
    "v_add_f32 v90, v90, v91\n"
    "v_mov_b32 v91, s40\n"
    "v_mov_b32 v6, 0\n"
    ".Ls_32_%=:\n"
    "ds_read_b32 v5, v6 offset:112\n"
    "s_waitcnt lgkmcnt(0)\n"
    "v_cmp_eq_u32 vcc, 0, v5\n"
    "s_cbranch_vccnz .Lr_33_%=\n"
    ".Lk_34_%=:\n"
    "v_readfirstlane_b32 s84, v5\n"
    "s_cmp_lg_u32 s84, 0\n"
    "s_cselect_b32 s56, s84, s56\n"
    ".Lt_35_%=:\n"
    "ds_read_b32 v5, v120 offset:144\n"
    "s_memrealtime s[58:59]\n"
    "s_waitcnt lgkmcnt(0)\n"
    "v_readfirstlane_b32 s55, v5\n"
    "s_min_u32 s55, s55, 120\n"
    "s_lshr_b32 s84, s55, 2\n"
    "s_add_u32 s57, s56, s84\n"
    "s_sub_u32 s58, s58, s57\n"
    "s_cmpk_lt_i32 s58, 563\n"
    "s_cbranch_scc1 .Lt_35_%=\n"
    "s_mov_b32 s56, s57\n"
    "s_mov_b32 s84, 0x05040100\n"
    "s_mov_b32 s85, 0x07060302\n"
    ".Lt_36_%=:\n"
    "s_memrealtime s[58:59]\n"
    "s_waitcnt lgkmcnt(0)\n"
    "s_sub_u32 s58, s58, s57\n"
    "s_cmpk_lt_i32 s58, 569\n"
    "s_cbranch_scc1 .Lt_36_%=\n"
    "buffer_load_dwordx4 v[8:11], v109, s[24:27], s96 offen nt\n"
    "s_xor_b32 s96, s96, 16384\n"
    ".Lt_37_%=:\n"
    "s_memrealtime s[58:59]\n"
    "s_waitcnt lgkmcnt(0)\n"
    "s_sub_u32 s58, s58, s57\n"
    "s_cmpk_lt_i32 s58, 583\n"
    "s_cbranch_scc1 .Lt_37_%=\n"
    "buffer_load_dwordx4 v[12:15], v109, s[24:27], s96 offen nt\n"
    "s_xor_b32 s96, s96, 49152\n"
    "s_cmp_lg_u32 s42, 0\n"
    "s_cbranch_scc1 .Ll_38_%=\n"
    "s_mov_b64 exec, 15\n"
    "buffer_store_dwordx2 v[90:91], v107, s[16:19], s89 offen sc1\n"
    "s_mov_b64 exec, -1\n"
    ".Ll_38_%=:\n"
    ".Lt_39_%=:\n"
    "s_memrealtime s[58:59]\n"
    "s_waitcnt lgkmcnt(0)\n"
    "s_sub_u32 s58, s58, s57\n"
    "s_cmpk_lt_i32 s58, 598\n"
    "s_cbranch_scc1 .Lt_39_%=\n"
    "buffer_load_dwordx4 v[16:19], v109, s[24:27], s96 offen nt\n"
    "s_xor_b32 s96, s96, 16384\n"
    ".Lt_40_%=:\n"
    "s_memrealtime s[58:59]\n"
    "s_waitcnt lgkmcnt(0)\n"
    "s_sub_u32 s58, s58, s57\n"
    "s_cmpk_lt_i32 s58, 612\n"
    "s_cbranch_scc1 .Lt_40_%=\n"
    "buffer_load_dwordx4 v[20:23], v109, s[24:27], s96 offen nt\n"
    "s_xor_b32 s96, s96, 114688\n"
    ".Lt_41_%=:\n"
    "s_memrealtime s[58:59]\n"
    "s_waitcnt lgkmcnt(0)\n"
    "s_sub_u32 s58, s58, s57\n"
    "s_cmpk_lt_i32 s58, 626\n"
    "s_cbranch_scc1 .Lt_41_%=\n"
    "buffer_load_dwordx4 v[24:27], v109, s[24:27], s96 offen nt\n"
    "s_xor_b32 s96, s96, 16384\n"
    ".Lt_42_%=:\n"
    "s_memrealtime s[58:59]\n"
    "s_waitcnt lgkmcnt(0)\n"
    "s_sub_u32 s58, s58, s57\n"
    "s_cmpk_lt_i32 s58, 641\n"
    "s_cbranch_scc1 .Lt_42_%=\n"
    "buffer_load_dwordx4 v[28:31], v109, s[24:27], s96 offen nt\n"
    "s_xor_b32 s96, s96, 49152\n"
    "buffer_load_dwordx2 v[96:97], v117, s[16:19], s98 offen sc1\n"
    ".Lt_43_%=:\n"
    "s_memrealtime s[58:59]\n"
    "s_waitcnt lgkmcnt(0)\n"
    "s_sub_u32 s58, s58, s57\n"
    "s_cmpk_lt_i32 s58, 655\n"
    "s_cbranch_scc1 .Lt_43_%=\n"
    "buffer_load_dwordx4 v[32:35], v109, s[24:27], s96 offen nt\n"
    "s_xor_b32 s96, s96, 16384\n"
    ".Lt_44_%=:\n"
    "s_memrealtime s[58:59]\n"
    "s_waitcnt lgkmcnt(0)\n"
    "s_sub_u32 s58, s58, s57\n"
    "s_cmpk_lt_i32 s58, 670\n"
    "s_cbranch_scc1 .Lt_44_%=\n"
    "buffer_load_dwordx4 v[36:39], v109, s[24:27], s96 offen nt\n"
    "s_waitcnt vmcnt(7)\n"
    "v_perm_b32 v40, v12, v8, s84\n"
    "v_perm_b32 v44, v12, v8, s85\n"
    "v_perm_b32 v48, v13, v9, s84\n"
    "v_perm_b32 v52, v13, v9, s85\n"
    "v_perm_b32 v56, v14, v10, s84\n"
    "v_perm_b32 v60, v14, v10, s85\n"
    "v_perm_b32 v64, v15, v11, s84\n"
    "v_perm_b32 v68, v15, v11, s85\n"
    "s_waitcnt vmcnt(5)\n"
    "v_perm_b32 v41, v20, v16, s84\n"
    "v_perm_b32 v45, v20, v16, s85\n"
    "v_perm_b32 v49, v21, v17, s84\n"
    "v_perm_b32 v53, v21, v17, s85\n"
    "v_perm_b32 v57, v22, v18, s84\n"
    "v_perm_b32 v61, v22, v18, s85\n"
    "v_perm_b32 v65, v23, v19, s84\n"
    "v_perm_b32 v69, v23, v19, s85\n"
    "s_waitcnt vmcnt(3)\n"
    "v_perm_b32 v42, v28, v24, s84\n"
    "v_perm_b32 v46, v28, v24, s85\n"
    "v_perm_b32 v50, v29, v25, s84\n"
    "v_perm_b32 v54, v29, v25, s85\n"
    "v_perm_b32 v58, v30, v26, s84\n"
    "v_perm_b32 v62, v30, v26, s85\n"
    "v_perm_b32 v66, v31, v27, s84\n"
    "v_perm_b32 v70, v31, v27, s85\n"
    "s_waitcnt vmcnt(2)\n"
    "s_cmp_lg_u32 s42, 0\n"
    "s_cbranch_scc1 .Lw_45_%=\n"
    "s_mov_b64 exec, 0xffff\n"
    ".Lc_46_%=:\n"
    "v_cmp_ne_u32 vcc, s40, v97\n"
    "s_cbranch_vccnz .Lr_47_%=\n"
    ".Lk_48_%=:\n"
    "v_max_f32_dpp v97, v96, v96 quad_perm:[1,0,3,2] row_mask:0xf bank_mask:0xf\n"
    "s_nop 1\n"
    "v_max_f32_dpp v97, v97, v97 quad_perm:[2,3,0,1] row_mask:0xf bank_mask:0xf\n"
    "s_nop 1\n"
    "v_max_f32_dpp v97, v97, v97 row_half_mirror row_mask:0xf bank_mask:0xf\n"
    "v_sub_f32 v96, v96, v97\n"
    "v_exp_f32 v96, v96\n"
    "s_nop 1\n"
    "v_add_f32_dpp v97, v96, v96 quad_perm:[1,0,3,2] row_mask:0xf bank_mask:0xf\n"
    "s_nop 1\n"
    "v_add_f32_dpp v97, v97, v97 quad_perm:[2,3,0,1] row_mask:0xf bank_mask:0xf\n"
    "s_nop 1\n"
    "v_add_f32_dpp v97, v97, v97 row_half_mirror row_mask:0xf bank_mask:0xf\n"
    "v_rcp_f32 v97, v97\n"
    "v_lshlrev_b32 v99, 2, v0\n"
    "v_mul_f32 v96, v96, v97\n"
    "ds_write_b32 v99, v96 offset:256\n"
    "ds_write_b32 v120, v121 offset:96\n"
    "s_mov_b64 exec, -1\n"
    ".Lw_45_%=:\n"
    ".Lt_49_%=:\n"
    "s_memrealtime s[58:59]\n"
    "s_waitcnt lgkmcnt(0)\n"
    "s_sub_u32 s58, s58, s56\n"
    "s_cmpk_lt_i32 s58, 725\n"
    "s_cbranch_scc1 .Lt_49_%=\n"
    "buffer_load_dwordx4 v[72:75], v110, s[16:19], s90 offen sc1\n"
    "buffer_load_dwordx4 v[76:79], v110, s[16:19], s91 offen sc1\n"
    "v_mov_b32 v6, 0\n"
    ".Ls_50_%=:\n"
    "ds_read_b32 v5, v6 offset:96\n"
    "ds_read_b128 v[84:87], v113 offset:256\n"
    "s_waitcnt lgkmcnt(0)\n"
    "v_cmp_ne_u32 vcc, 1, v5\n"
    "s_cbranch_vccnz .Lr_51_%=\n"
    ".Lk_52_%=:\n"
    ".Lo_53_%=:\n"
    "s_waitcnt vmcnt(1)\n"
    "ds_write2_b32 v111, v72, v74 offset0:0 offset1:1\n"
    "s_waitcnt vmcnt(0)\n"
    "ds_write2_b32 v111, v76, v78 offset0:160 offset1:161\n"
    "ds_read_b128 v[80:83], v112\n"
    "v_perm_b32 v43, v36, v32, s84\n"
    "v_perm_b32 v47, v36, v32, s85\n"
    "v_perm_b32 v51, v37, v33, s84\n"
    "v_perm_b32 v55, v37, v33, s85\n"
    "v_perm_b32 v59, v38, v34, s84\n"
    "v_perm_b32 v63, v38, v34, s85\n"
    "v_perm_b32 v67, v39, v35, s84\n"
    "v_perm_b32 v71, v39, v35, s85\n"
    "v_xor_b32 v5, s40, v73\n"
    "v_xor_b32 v6, s40, v75\n"
    "v_xor_b32 v77, s40, v77\n"
    "v_xor_b32 v79, s40, v79\n"
    "v_or3_b32 v5, v5, v6, v77\n"
    "v_or_b32 v5, v5, v79\n"
    "v_cmp_ne_u32 vcc, 0, v5\n"
    "s_cbranch_vccnz .Lr_54_%=\n"
    ".Lk_55_%=:\n"
    "s_waitcnt lgkmcnt(0)\n"
    "v_mfma_f32_16x16x32_bf16 v[8:11], v[80:83], v[40:43], 0\n"
    "v_mfma_f32_16x16x32_bf16 v[12:15], v[80:83], v[44:47], 0\n"
    "v_mfma_f32_16x16x32_bf16 v[16:19], v[80:83], v[48:51], 0\n"
    "v_mfma_f32_16x16x32_bf16 v[20:23], v[80:83], v[52:55], 0\n"
    "s_nop 4\n"
    "v_mul_f32 v88, v8, v84\n"
    "v_fmac_f32 v88, v9, v85\n"
    "v_mfma_f32_16x16x32_bf16 v[24:27], v[80:83], v[56:59], 0\n"
    "v_fmac_f32 v88, v10, v86\n"
    "v_fmac_f32 v88, v11, v87\n"
    "v_mul_f32 v89, v12, v84\n"
    "v_fmac_f32 v89, v13, v85\n"
    "v_mfma_f32_16x16x32_bf16 v[28:31], v[80:83], v[60:63], 0\n"
    "v_fmac_f32 v89, v14, v86\n"
    "v_fmac_f32 v89, v15, v87\n"
    "v_mul_f32 v90, v16, v84\n"
    "v_fmac_f32 v90, v17, v85\n"
    "v_mfma_f32_16x16x32_bf16 v[32:35], v[80:83], v[64:67], 0\n"
    "v_fmac_f32 v90, v18, v86\n"
    "v_fmac_f32 v90, v19, v87\n"
    "v_mul_f32 v91, v20, v84\n"
    "v_fmac_f32 v91, v21, v85\n"
    "v_mfma_f32_16x16x32_bf16 v[36:39], v[80:83], v[68:71], 0\n"
    "v_fmac_f32 v91, v22, v86\n"
    "v_fmac_f32 v91, v23, v87\n"
    "ds_write_b128 v114, v[88:91]\n"
    "v_mul_f32 v92, v24, v84\n"
    "v_fmac_f32 v92, v25, v85\n"
    "v_fmac_f32 v92, v26, v86\n"
    "v_fmac_f32 v92, v27, v87\n"
    "v_mul_f32 v93, v28, v84\n"
    "v_fmac_f32 v93, v29, v85\n"
    "v_fmac_f32 v93, v30, v86\n"
    "v_fmac_f32 v93, v31, v87\n"
    "v_mul_f32 v94, v32, v84\n"
    "v_fmac_f32 v94, v33, v85\n"
    "v_fmac_f32 v94, v34, v86\n"
    "v_fmac_f32 v94, v35, v87\n"
    "v_mul_f32 v95, v36, v84\n"
    "v_fmac_f32 v95, v37, v85\n"
    "v_fmac_f32 v95, v38, v86\n"
    "v_fmac_f32 v95, v39, v87\n"
    "ds_write_b128 v114, v[92:95] offset:256\n"
    "s_waitcnt lgkmcnt(0)\n"
    "s_barrier\n"
    "s_cmp_gt_u32 s42, 0\n"
    "s_cbranch_scc1 .Le_56_%=\n"
    "s_memrealtime s[58:59]\n"
    "s_waitcnt lgkmcnt(0)\n"
    "s_sub_u32 s58, s58, s56\n"
    "s_addk_i32 s58, -774\n"
    "s_ashr_i32 s58, s58, 1\n"
    "s_add_u32 s56, s56, s58\n"
    ".Lt_57_%=:\n"
    "s_memrealtime s[58:59]\n"
    "s_waitcnt lgkmcnt(0)\n"
    "s_sub_u32 s58, s58, s56\n"
    "s_cmpk_lt_i32 s58, 811\n"
    "s_cbranch_scc1 .Lt_57_%=\n"
    "buffer_load_dwordx4 v[8:11], v115, s[16:19], s93 offen sc1\n"
    "buffer_load_dwordx4 v[12:15], v115, s[16:19], s94 offen sc1\n"
    ".Lc_58_%=:\n"
    "s_waitcnt vmcnt(0)\n"
    "v_add_f32 v8, v8, v12\n"
    "v_add_f32 v10, v10, v14\n"
    "v_xor_b32 v5, s40, v9\n"
    "v_xor_b32 v6, s40, v11\n"
    "v_permlane16_swap_b32 v8, v10\n"
    "v_add_f32 v8, v8, v10\n"
    "v_mov_b32 v10, v8\n"
    "v_xor_b32 v13, s40, v13\n"
    "v_xor_b32 v15, s40, v15\n"
    "v_permlane32_swap_b32 v8, v10\n"
    "v_add_f32 v8, v8, v10\n"
    "v_or3_b32 v5, v5, v6, v13\n"
    "v_or_b32 v5, v5, v15\n"
    "v_add_f32_dpp v8, v8, v8 row_ror:8 row_mask:0xf bank_mask:0xf\n"
    "v_cmp_ne_u32 vcc, 0, v5\n"
    "s_cbranch_vccnz .Lr_59_%=\n"
    ".Lk_60_%=:\n"
    "v_add_f32_dpp v8, v8, v8 row_ror:4 row_mask:0xf bank_mask:0xf\n"
    "s_mov_b64 exec, 0xf000f\n"
    "v_cvt_pk_bf16_f32 v8, v8, v8\n"
    "buffer_store_short v8, v116, s[28:31], s95 offen\n"
    ".Le_56_%=:\n"
    "s_endpgm\n"
    ".Lr_20_%=:\n"
    "s_sub_i32 s41, s41, 1\n"
    "s_cmp_gt_i32 s41, 0\n"
    "s_cbranch_scc0 .Lk_21_%=\n"
    "s_branch .Ls_19_%=\n"
    ".Lr_24_%=:\n"
    "s_sub_i32 s41, s41, 1\n"
    "s_cmp_gt_i32 s41, 0\n"
    "s_cbranch_scc0 .Lk_25_%=\n"
    "s_sleep 4\n"
    "buffer_load_dwordx4 v[72:75], v123, s[16:19], s88 offen sc1\n"
    "buffer_load_dwordx4 v[76:79], v123, s[16:19], s88 offen offset:1024 sc1\n"
    "s_branch .Lc_23_%=\n"
    ".Lr_27_%=:\n"
    "s_sub_i32 s41, s41, 1\n"
    "s_cmp_gt_i32 s41, 0\n"
    "s_cbranch_scc0 .Lk_28_%=\n"
    "s_branch .Ls_26_%=\n"
    ".Lr_30_%=:\n"
    "s_sub_i32 s41, s41, 1\n"
    "s_cmp_gt_i32 s41, 0\n"
    "s_cbranch_scc0 .Lk_31_%=\n"
    "s_branch .Ls_29_%=\n"
    ".Lr_33_%=:\n"
    "s_sub_i32 s41, s41, 1\n"
    "s_cmp_gt_i32 s41, 0\n"
    "s_cbranch_scc0 .Lk_34_%=\n"
    "s_sleep 1\n"
    "s_branch .Ls_32_%=\n"
    ".Lr_47_%=:\n"
    "s_sub_i32 s41, s41, 1\n"
    "s_cmp_gt_i32 s41, 0\n"
    "s_cbranch_scc0 .Lk_48_%=\n"
    "s_sleep 4\n"
    "buffer_load_dwordx2 v[96:97], v117, s[16:19], s98 offen sc1\n"
    "s_waitcnt vmcnt(0)\n"
    "s_branch .Lc_46_%=\n"
    ".Lr_51_%=:\n"
    "s_sub_i32 s41, s41, 1\n"
    "s_cmp_gt_i32 s41, 0\n"
    "s_cbranch_scc0 .Lk_52_%=\n"
    "s_branch .Ls_50_%=\n"
    ".Lr_54_%=:\n"
    "s_sub_i32 s41, s41, 1\n"
    "s_cmp_gt_i32 s41, 0\n"
    "s_cbranch_scc0 .Lk_55_%=\n"
    "s_waitcnt lgkmcnt(0)\n"
    "buffer_load_dwordx4 v[72:75], v110, s[16:19], s90 offen sc1\n"
    "buffer_load_dwordx4 v[76:79], v110, s[16:19], s91 offen sc1\n"
    "s_branch .Lo_53_%=\n"
    ".Lr_59_%=:\n"
    "s_sub_i32 s41, s41, 1\n"
    "s_cmp_gt_i32 s41, 0\n"
    "s_cbranch_scc0 .Lk_60_%=\n"
    "buffer_load_dwordx4 v[8:11], v115, s[16:19], s93 offen sc1\n"
    "buffer_load_dwordx4 v[12:15], v115, s[16:19], s94 offen sc1\n"
    "s_branch .Lc_58_%=\n"
    ".LA_2_%=:\n"
    "v_and_b32 v80, 1, v0\n"
    "v_cmp_eq_u32 vcc, 0, v80\n"
    "v_mov_b32 v80, 0x01000c0c\n"
    "v_mov_b32 v81, 0x0c0c0100\n"
    "v_mov_b32 v82, 0x03020c0c\n"
    "v_mov_b32 v83, 0x0c0c0302\n"
    "v_cndmask_b32 v84, v80, v81, vcc\n"
    "v_cndmask_b32 v85, v82, v83, vcc\n"
    "v_bfe_u32 v86, v0, 1, 2\n"
    "v_mov_b32 v87, 0x0c0c0c0c\n"
    "v_cmp_eq_u32 vcc, 0, v86\n"
    "s_nop 1\n"
    "v_cndmask_b32 v72, v87, v84, vcc\n"
    "v_cndmask_b32 v76, v87, v85, vcc\n"
    "v_cmp_eq_u32 vcc, 1, v86\n"
    "s_nop 1\n"
    "v_cndmask_b32 v73, v87, v84, vcc\n"
    "v_cndmask_b32 v77, v87, v85, vcc\n"
    "v_cmp_eq_u32 vcc, 2, v86\n"
    "s_nop 1\n"
    "v_cndmask_b32 v74, v87, v84, vcc\n"
    "v_cndmask_b32 v78, v87, v85, vcc\n"
    "v_cmp_eq_u32 vcc, 3, v86\n"
    "s_nop 1\n"
    "v_cndmask_b32 v75, v87, v84, vcc\n"
    "v_cndmask_b32 v79, v87, v85, vcc\n"
    "s_lshl_b32 s84, s42, 2\n"
    "v_mov_b32 v120, s84\n"
    "v_mov_b32 v121, 1\n"
    "v_and_b32 v122, 3, v0\n"
    "v_lshlrev_b32 v122, 2, v122\n"
    "v_and_b32 v123, 63, v0\n"
    "v_lshlrev_b32 v123, 4, v123\n"
    "v_lshlrev_b32 v1, 4, v0\n"
    "v_and_b32 v86, 4, v0\n"
    "v_cmp_eq_u32 vcc, 0, v86\n"
    "s_lshr_b32 s84, s46, 2\n"
    "s_lshl_b32 s84, s84, 14\n"
    "s_lshl2_add_u32 s85, s50, s45\n"
    "s_add_u32 s84, s84, s48\n"
    "v_cndmask_b32 v84, v80, v81, vcc\n"
    "v_cndmask_b32 v85, v82, v83, vcc\n"
    "v_and_b32 v86, 8, v0\n"
    "v_cmp_eq_u32 vcc, 0, v86\n"
    "s_lshl_b32 s85, s85, 9\n"
    "s_add_u32 s84, s84, s85\n"
    "v_cndmask_b32 v124, v87, v84, vcc\n"
    "v_cndmask_b32 v125, v84, v87, vcc\n"
    "v_cndmask_b32 v126, v87, v85, vcc\n"
    "v_cndmask_b32 v127, v85, v87, vcc\n"
    "s_add_u32 s93, s84, 32768\n"
    "v_bfe_u32 v2, v0, 5, 1\n"
    "v_and_b32 v3, 1, v0\n"
    "v_lshl_or_b32 v3, v2, 1, v3\n"
    "v_bfe_u32 v80, v0, 1, 4\n"
    "v_lshl_or_b32 v3, v3, 4, v80\n"
    "v_lshlrev_b32 v3, 4, v3\n"
    "v_and_b32 v80, 31, v0\n"
    "v_lshlrev_b32 v80, 4, v80\n"
    "v_lshl_or_b32 v2, v2, 13, v80\n"
    "s_waitcnt vmcnt(8)\n"
    "v_perm_b32 v40, v4, v4, v72\n"
    "v_perm_b32 v41, v4, v4, v73\n"
    "v_perm_b32 v42, v4, v4, v74\n"
    "v_perm_b32 v43, v4, v4, v75\n"
    "v_perm_b32 v44, v4, v4, v76\n"
    "v_perm_b32 v45, v4, v4, v77\n"
    "v_perm_b32 v46, v4, v4, v78\n"
    "v_perm_b32 v47, v4, v4, v79\n"
    "v_perm_b32 v48, v5, v5, v72\n"
    "v_perm_b32 v49, v5, v5, v73\n"
    "v_perm_b32 v50, v5, v5, v74\n"
    "v_perm_b32 v51, v5, v5, v75\n"
    "v_perm_b32 v52, v5, v5, v76\n"
    "v_perm_b32 v53, v5, v5, v77\n"
    "v_perm_b32 v54, v5, v5, v78\n"
    "v_perm_b32 v55, v5, v5, v79\n"
    "v_perm_b32 v56, v6, v6, v72\n"
    "v_perm_b32 v57, v6, v6, v73\n"
    "v_perm_b32 v58, v6, v6, v74\n"
    "v_perm_b32 v59, v6, v6, v75\n"
    "v_perm_b32 v60, v6, v6, v76\n"
    "v_perm_b32 v61, v6, v6, v77\n"
    "v_perm_b32 v62, v6, v6, v78\n"
    "v_perm_b32 v63, v6, v6, v79\n"
    "v_perm_b32 v64, v7, v7, v72\n"
    "v_perm_b32 v65, v7, v7, v73\n"
    "v_perm_b32 v66, v7, v7, v74\n"
    "v_perm_b32 v67, v7, v7, v75\n"
    "v_perm_b32 v68, v7, v7, v76\n"
    "v_perm_b32 v69, v7, v7, v77\n"
    "v_perm_b32 v70, v7, v7, v78\n"
    "v_perm_b32 v71, v7, v7, v79\n"
    "s_waitcnt lgkmcnt(0)\n"
    "s_add_u32 s40, s40, 1\n"
    "v_mov_b32 v89, s40\n"
    "v_mov_b32 v91, s40\n"
    "ds_write_b32 v120, v89 offset:80\n"
    "s_or_b32 s84, s46, s42\n"
    "s_cbranch_scc1 .Lp_61_%=\n"
    "s_lshl_b32 s84, s2, 7\n"
    "s_addk_i32 s84, 20480\n"
    "v_mov_b32 v88, s56\n"
    "s_mov_b64 exec, 1\n"
    "buffer_store_dwordx2 v[88:89], off, s[16:19], s84 sc1\n"
    "s_mov_b64 exec, -1\n"
    ".Lp_61_%=:\n"
    "s_waitcnt vmcnt(7)\n"
    "v_mfma_f32_16x16x32_bf16 v[4:7], v[40:43], v[8:11], 0\n"
    "s_waitcnt vmcnt(6)\n"
    "v_mfma_f32_16x16x32_bf16 v[4:7], v[44:47], v[12:15], v[4:7]\n"
    "s_waitcnt vmcnt(5)\n"
    "v_mfma_f32_16x16x32_bf16 v[4:7], v[48:51], v[16:19], v[4:7]\n"
    "s_waitcnt vmcnt(4)\n"
    "v_mfma_f32_16x16x32_bf16 v[4:7], v[52:55], v[20:23], v[4:7]\n"
    "s_waitcnt vmcnt(3)\n"
    "v_mfma_f32_16x16x32_bf16 v[4:7], v[56:59], v[24:27], v[4:7]\n"
    "s_waitcnt vmcnt(2)\n"
    "v_mfma_f32_16x16x32_bf16 v[4:7], v[60:63], v[28:31], v[4:7]\n"
    "s_waitcnt vmcnt(1)\n"
    "v_mfma_f32_16x16x32_bf16 v[4:7], v[64:67], v[32:35], v[4:7]\n"
    "s_waitcnt vmcnt(0)\n"
    "v_mfma_f32_16x16x32_bf16 v[4:7], v[68:71], v[36:39], v[4:7]\n"
    "s_nop 7\n"
    "ds_write_b128 v1, v[4:7] offset:3584\n"
    "ds_write_b32 v120, v121 offset:0\n"
    "s_cmp_lg_u32 s42, 0\n"
    "s_cbranch_scc1 .La_62_%=\n"
    ".Ls_63_%=:\n"
    "ds_read_b32 v92, v122 offset:0\n"
    "ds_read_b128 v[72:75], v3 offset:3584\n"
    "ds_read_b128 v[76:79], v3 offset:4608\n"
    "ds_read_b128 v[80:83], v3 offset:5632\n"
    "ds_read_b128 v[84:87], v3 offset:6656\n"
    "s_waitcnt lgkmcnt(0)\n"
    "v_cmp_ne_u32 vcc, 1, v92\n"
    "s_cbranch_vccnz .Lr_64_%=\n"
    ".Lk_65_%=:\n"
    "v_pk_add_f32 v[72:73], v[72:73], v[76:77]\n"
    "v_pk_add_f32 v[74:75], v[74:75], v[78:79]\n"
    "v_pk_add_f32 v[80:81], v[80:81], v[84:85]\n"
    "v_pk_add_f32 v[82:83], v[82:83], v[86:87]\n"
    "v_pk_add_f32 v[72:73], v[72:73], v[80:81]\n"
    "v_pk_add_f32 v[74:75], v[74:75], v[82:83]\n"
    "v_cvt_pk_bf16_f32 v88, v72, v73\n"
    "v_cvt_pk_bf16_f32 v90, v74, v75\n"
    "buffer_store_dwordx4 v[88:91], v2, s[16:19], s93 offen sc1\n"
    ".La_62_%=:\n"
    "v_lshrrev_b32 v4, 4, v0\n"
    "v_lshlrev_b32 v4, 7, v4\n"
    "v_lshl_or_b32 v128, v122, 3, v4\n"
    "s_mul_i32 s84, s42, 2304\n"
    "v_and_b32 v4, 0x30, v0\n"
    "v_add3_u32 v129, s84, v123, v4\n"
    "v_bfe_u32 v5, v0, 4, 2\n"
    "s_lshl_b32 s84, s43, 2\n"
    "v_or_b32 v4, s84, v5\n"
    "v_and_b32 v4, 7, v4\n"
    "v_lshrrev_b32 v6, 1, v122\n"
    "v_xor_b32 v4, v4, v6\n"
    "v_lshlrev_b32 v4, 5, v4\n"
    "v_xor_b32 v6, 32, v4\n"
    "v_bfe_u32 v7, v0, 2, 2\n"
    "v_mul_u32_u24 v7, 272, v7\n"
    "v_lshl_add_u32 v7, v5, 2, v7\n"
    "s_and_b32 s84, s42, 1\n"
    "s_mul_i32 s84, s84, 1088\n"
    "s_lshr_b32 s85, s42, 1\n"
    "s_lshl_b32 s85, s85, 4\n"
    "s_add_u32 s84, s84, s85\n"
    "v_add3_u32 v130, v4, v7, s84\n"
    "v_add3_u32 v131, v6, v7, s84\n"
    "v_mul_u32_u24 v4, 8704, v5\n"
    "v_and_b32 v5, 15, v0\n"
    "v_lshl_add_u32 v132, v5, 3, v4\n"
    "v_bfe_u32 v4, v0, 2, 1\n"
    "v_lshl_or_b32 v4, v4, 8, v122\n"
    "v_bfe_u32 v5, v0, 3, 4\n"
    "v_lshl_or_b32 v4, v5, 4, v4\n"
    "v_bfe_u32 v5, v0, 7, 1\n"
    "v_lshl_or_b32 v4, v5, 10, v4\n"
    "v_add_u32 v133, 22016, v4\n"
    "v_lshrrev_b32 v134, 1, v123\n"
    "s_mul_i32 s84, s43, 34816\n"
    "s_lshl_b32 s85, s44, 9\n"
    "s_lshl_b32 s86, s42, 7\n"
    "s_add_u32 s84, s84, s85\n"
    "s_add_u32 s84, s84, s86\n"
    "s_add_u32 s84, s84, s47\n"
    "s_add_u32 s95, s84, 163840\n"
    "s_mul_i32 s84, s50, 69632\n"
    "s_mul_i32 s85, s49, 2176\n"
    "s_add_u32 s84, s84, s48\n"
    "s_lshl_b32 s86, s42, 9\n"
    "s_add_u32 s84, s84, s85\n"
    "s_add_u32 s84, s84, s86\n"
    "s_add_u32 s96, s84, 442368\n"
    "v_mov_b32 v3, s40\n"
    "s_bfe_u32 s84, s44, 0x10000\n"
    "s_lshl_b32 s84, s84, 10\n"
    "s_or_b32 s51, s51, s84\n"
    "s_bfe_u32 s84, s44, 0x20001\n"
    "s_lshl_b32 s84, s84, 12\n"
    "s_or_b32 s51, s51, s84\n"
    "s_bfe_u32 s84, s42, 0x10000\n"
    "s_lshl_b32 s84, s84, 16\n"
    "s_or_b32 s51, s51, s84\n"
    "s_bfe_u32 s84, s42, 0x10001\n"
    "s_lshl_b32 s84, s84, 18\n"
    "s_or_b32 s51, s51, s84\n"
    "s_lshr_b32 s85, s44, 3\n"
    "s_lshl1_add_u32 s85, s43, s85\n"
    "s_bfe_u32 s52, s85, 0x10000\n"
    "s_lshl_b32 s52, s52, 11\n"
    "s_bfe_u32 s84, s85, 0x20001\n"
    "s_lshl_b32 s84, s84, 14\n"
    "s_or_b32 s52, s52, s84\n"
    "s_bfe_u32 s84, s85, 0x10003\n"
    "s_lshl_b32 s84, s84, 17\n"
    "s_or_b32 s52, s52, s84\n"
    "s_mov_b32 s53, 0x2c800\n"
    "s_not_b32 s54, s53\n"
    "v_or_b32 v1, s51, v123\n"
    "s_cmp_lg_u32 s42, 1\n"
    "s_cbranch_scc1 .La_67_%=\n"
    ".Lt_68_%=:\n"
    "s_memrealtime s[58:59]\n"
    "s_waitcnt lgkmcnt(0)\n"
    "s_sub_u32 s58, s58, s56\n"
    "s_cmpk_lt_i32 s58, 190\n"
    "s_cbranch_scc1 .Lt_68_%=\n"
    "s_mov_b64 exec, 0xff\n"
    "v_lshlrev_b32 v6, 3, v123\n"
    "s_movk_i32 s84, 20480\n"
    "buffer_load_dwordx2 v[4:5], v6, s[16:19], s84 offen sc1\n"
    "s_waitcnt vmcnt(0)\n"
    "v_cmp_ne_u32 vcc, s40, v5\n"
    "s_cbranch_vccnz .Lr_66_%=\n"
    "v_subrev_u32 v4, s56, v4\n"
    "s_nop 1\n"
    "v_add_u32_dpp v4, v4, v4 quad_perm:[1,0,3,2] row_mask:0xf bank_mask:0xf\n"
    "s_nop 1\n"
    "v_add_u32_dpp v4, v4, v4 quad_perm:[2,3,0,1] row_mask:0xf bank_mask:0xf\n"
    "s_nop 1\n"
    "v_add_u32_dpp v4, v4, v4 row_half_mirror row_mask:0xf bank_mask:0xf\n"
    "s_nop 0\n"
    "v_readfirstlane_b32 s84, v4\n"
    "s_ashr_i32 s84, s84, 3\n"
    "s_min_i32 s84, s84, 64\n"
    "s_max_i32 s84, s84, -64\n"
    "s_add_u32 s56, s56, s84\n"
    ".Lr_66_%=:\n"
    "s_or_b32 s56, s56, 1\n"
    "v_mov_b32 v4, s56\n"
    "ds_write_b32 v120, v4 offset:108\n"
    "s_mov_b64 exec, -1\n"
    ".La_67_%=:\n"
    "v_mov_b32 v5, 0\n"
    ".Ls_69_%=:\n"
    "ds_read_b32 v4, v5 offset:112\n"
    "s_waitcnt lgkmcnt(0)\n"
    "v_cmp_eq_u32 vcc, 0, v4\n"
    "s_cbranch_vccnz .Lr_70_%=\n"
    ".Lk_71_%=:\n"
    "v_readfirstlane_b32 s84, v4\n"
    "s_cmp_lg_u32 s84, 0\n"
    "s_cselect_b32 s56, s84, s56\n"
    ".Lt_72_%=:\n"
    "ds_read_b32 v5, v120 offset:128\n"
    "s_memrealtime s[58:59]\n"
    "s_waitcnt lgkmcnt(0)\n"
    "v_readfirstlane_b32 s55, v5\n"
    "s_min_u32 s55, s55, 120\n"
    "s_lshr_b32 s84, s55, 2\n"
    "s_add_u32 s57, s56, s84\n"
    "s_sub_u32 s58, s58, s57\n"
    "s_cmpk_lt_i32 s58, 318\n"
    "s_cbranch_scc1 .Lt_72_%=\n"
    ".Lt_73_%=:\n"
    "s_memrealtime s[58:59]\n"
    "s_waitcnt lgkmcnt(0)\n"
    "s_sub_u32 s58, s58, s57\n"
    "s_cmpk_lt_i32 s58, 324\n"
    "s_cbranch_scc1 .Lt_73_%=\n"
    "buffer_load_dwordx4 v[8:11], v1, s[20:23], s52 offen nt\n"
    "s_or_b32 s52, s52, s54\n"
    "s_add_u32 s52, s52, 1\n"
    "s_and_b32 s52, s52, s53\n"
    ".Lt_74_%=:\n"
    "s_memrealtime s[58:59]\n"
    "s_waitcnt lgkmcnt(0)\n"
    "s_sub_u32 s58, s58, s57\n"
    "s_cmpk_lt_i32 s58, 339\n"
    "s_cbranch_scc1 .Lt_74_%=\n"
    "buffer_load_dwordx4 v[12:15], v1, s[20:23], s52 offen nt\n"
    "s_or_b32 s52, s52, s54\n"
    "s_add_u32 s52, s52, 1\n"
    "s_and_b32 s52, s52, s53\n"
    ".Lt_75_%=:\n"
    "s_memrealtime s[58:59]\n"
    "s_waitcnt lgkmcnt(0)\n"
    "s_sub_u32 s58, s58, s57\n"
    "s_cmpk_lt_i32 s58, 353\n"
    "s_cbranch_scc1 .Lt_75_%=\n"
    "buffer_load_dwordx4 v[16:19], v1, s[20:23], s52 offen nt\n"
    "s_or_b32 s52, s52, s54\n"
    "s_add_u32 s52, s52, 1\n"
    "s_and_b32 s52, s52, s53\n"
    ".Lt_76_%=:\n"
    "s_memrealtime s[58:59]\n"
    "s_waitcnt lgkmcnt(0)\n"
    "s_sub_u32 s58, s58, s57\n"
    "s_cmpk_lt_i32 s58, 368\n"
    "s_cbranch_scc1 .Lt_76_%=\n"
    "buffer_load_dwordx4 v[20:23], v1, s[20:23], s52 offen nt\n"
    "s_or_b32 s52, s52, s54\n"
    "s_add_u32 s52, s52, 1\n"
    "s_and_b32 s52, s52, s53\n"
    ".Lt_77_%=:\n"
    "s_memrealtime s[58:59]\n"
    "s_waitcnt lgkmcnt(0)\n"
    "s_sub_u32 s58, s58, s57\n"
    "s_cmpk_lt_i32 s58, 382\n"
    "s_cbranch_scc1 .Lt_77_%=\n"
    "buffer_load_dwordx4 v[24:27], v1, s[20:23], s52 offen nt\n"
    "s_or_b32 s52, s52, s54\n"
    "s_add_u32 s52, s52, 1\n"
    "s_and_b32 s52, s52, s53\n"
    ".Lt_78_%=:\n"
    "s_memrealtime s[58:59]\n"
    "s_waitcnt lgkmcnt(0)\n"
    "s_sub_u32 s58, s58, s57\n"
    "s_cmpk_lt_i32 s58, 397\n"
    "s_cbranch_scc1 .Lt_78_%=\n"
    "buffer_load_dwordx4 v[28:31], v1, s[20:23], s52 offen nt\n"
    "s_getreg_b32 s84, hwreg(7, 0, 4)\n"
    "s_cmp_gt_u32 s84, 5\n"
    "s_cselect_b32 s84, 5, -1\n"
    "s_add_i32 s55, s55, s84\n"
    "s_max_i32 s55, s55, 0\n"
    "s_lshr_b32 s84, s55, 2\n"
    "s_add_u32 s57, s56, s84\n"
    "s_or_b32 s52, s52, s54\n"
    "s_add_u32 s52, s52, 1\n"
    "s_and_b32 s52, s52, s53\n"
    ".Lt_79_%=:\n"
    "s_memrealtime s[58:59]\n"
    "s_waitcnt lgkmcnt(0)\n"
    "s_sub_u32 s58, s58, s57\n"
    "s_cmpk_lt_i32 s58, 412\n"
    "s_cbranch_scc1 .Lt_79_%=\n"
    "buffer_load_dwordx4 v[32:35], v1, s[20:23], s52 offen nt\n"
    "s_getreg_b32 s84, hwreg(7, 0, 4)\n"
    "s_cmp_gt_u32 s84, 5\n"
    "s_cselect_b32 s84, 5, -1\n"
    "s_add_i32 s55, s55, s84\n"
    "s_max_i32 s55, s55, 0\n"
    "s_lshr_b32 s84, s55, 2\n"
    "s_add_u32 s57, s56, s84\n"
    "s_or_b32 s52, s52, s54\n"
    "s_add_u32 s52, s52, 1\n"
    "s_and_b32 s52, s52, s53\n"
    ".Lt_80_%=:\n"
    "s_memrealtime s[58:59]\n"
    "s_waitcnt lgkmcnt(0)\n"
    "s_sub_u32 s58, s58, s57\n"
    "s_cmpk_lt_i32 s58, 426\n"
    "s_cbranch_scc1 .Lt_80_%=\n"
    "buffer_load_dwordx4 v[36:39], v1, s[20:23], s52 offen nt\n"
    "s_getreg_b32 s84, hwreg(7, 0, 4)\n"
    "s_cmp_gt_u32 s84, 5\n"
    "s_cselect_b32 s84, 5, -1\n"
    "s_add_i32 s55, s55, s84\n"
    "s_max_i32 s55, s55, 0\n"
    "s_lshr_b32 s84, s55, 2\n"
    "s_add_u32 s57, s56, s84\n"
    "s_or_b32 s52, s52, s54\n"
    "s_add_u32 s52, s52, 1\n"
    "s_and_b32 s52, s52, s53\n"
    ".Lt_81_%=:\n"
    "s_memrealtime s[58:59]\n"
    "s_waitcnt lgkmcnt(0)\n"
    "s_sub_u32 s58, s58, s57\n"
    "s_cmpk_lt_i32 s58, 441\n"
    "s_cbranch_scc1 .Lt_81_%=\n"
    "buffer_load_dwordx4 v[40:43], v1, s[20:23], s52 offen nt\n"
    "s_getreg_b32 s84, hwreg(7, 0, 4)\n"
    "s_cmp_gt_u32 s84, 5\n"
    "s_cselect_b32 s84, 5, -1\n"
    "s_add_i32 s55, s55, s84\n"
    "s_max_i32 s55, s55, 0\n"
    "s_lshr_b32 s84, s55, 2\n"
    "s_add_u32 s57, s56, s84\n"
    "s_or_b32 s52, s52, s54\n"
    "s_add_u32 s52, s52, 1\n"
    "s_and_b32 s52, s52, s53\n"
    "s_waitcnt vmcnt(8)\n"
    ".Lt_82_%=:\n"
    "s_memrealtime s[58:59]\n"
    "s_waitcnt lgkmcnt(0)\n"
    "s_sub_u32 s58, s58, s57\n"
    "s_cmpk_lt_i32 s58, 456\n"
    "s_cbranch_scc1 .Lt_82_%=\n"
    "buffer_load_dwordx4 v[44:47], v1, s[20:23], s52 offen nt\n"
    "s_getreg_b32 s84, hwreg(7, 0, 4)\n"
    "s_cmp_gt_u32 s84, 5\n"
    "s_cselect_b32 s84, 5, -1\n"
    "s_add_i32 s55, s55, s84\n"
    "s_max_i32 s55, s55, 0\n"
    "s_lshr_b32 s84, s55, 2\n"
    "s_add_u32 s57, s56, s84\n"
    "s_or_b32 s52, s52, s54\n"
    "s_add_u32 s52, s52, 1\n"
    "s_and_b32 s52, s52, s53\n"
    "s_waitcnt vmcnt(8)\n"
    ".Lt_83_%=:\n"
    "s_memrealtime s[58:59]\n"
    "s_waitcnt lgkmcnt(0)\n"
    "s_sub_u32 s58, s58, s57\n"
    "s_cmpk_lt_i32 s58, 470\n"
    "s_cbranch_scc1 .Lt_83_%=\n"
    "buffer_load_dwordx4 v[48:51], v1, s[20:23], s52 offen nt\n"
    "s_getreg_b32 s84, hwreg(7, 0, 4)\n"
    "s_cmp_gt_u32 s84, 5\n"
    "s_cselect_b32 s84, 5, -1\n"
    "s_add_i32 s55, s55, s84\n"
    "s_max_i32 s55, s55, 0\n"
    "s_lshr_b32 s84, s55, 2\n"
    "s_add_u32 s57, s56, s84\n"
    "s_or_b32 s52, s52, s54\n"
    "s_add_u32 s52, s52, 1\n"
    "s_and_b32 s52, s52, s53\n"
    "s_waitcnt vmcnt(8)\n"
    ".Lt_84_%=:\n"
    "s_memrealtime s[58:59]\n"
    "s_waitcnt lgkmcnt(0)\n"
    "s_sub_u32 s58, s58, s57\n"
    "s_cmpk_lt_i32 s58, 485\n"
    "s_cbranch_scc1 .Lt_84_%=\n"
    "buffer_load_dwordx4 v[52:55], v1, s[20:23], s52 offen nt\n"
    "s_getreg_b32 s84, hwreg(7, 0, 4)\n"
    "s_cmp_gt_u32 s84, 5\n"
    "s_cselect_b32 s84, 5, -1\n"
    "s_add_i32 s55, s55, s84\n"
    "s_max_i32 s55, s55, 0\n"
    "s_lshr_b32 s84, s55, 2\n"
    "s_add_u32 s57, s56, s84\n"
    "s_or_b32 s52, s52, s54\n"
    "s_add_u32 s52, s52, 1\n"
    "s_and_b32 s52, s52, s53\n"
    "s_waitcnt vmcnt(8)\n"
    ".Lt_85_%=:\n"
    "s_memrealtime s[58:59]\n"
    "s_waitcnt lgkmcnt(0)\n"
    "s_sub_u32 s58, s58, s57\n"
    "s_cmpk_lt_i32 s58, 499\n"
    "s_cbranch_scc1 .Lt_85_%=\n"
    "buffer_load_dwordx4 v[56:59], v1, s[20:23], s52 offen nt\n"
    "s_getreg_b32 s84, hwreg(7, 0, 4)\n"
    "s_cmp_gt_u32 s84, 5\n"
    "s_cselect_b32 s84, 5, -1\n"
    "s_add_i32 s55, s55, s84\n"
    "s_max_i32 s55, s55, 0\n"
    "s_lshr_b32 s84, s55, 2\n"
    "s_add_u32 s57, s56, s84\n"
    "v_mov_b32 v5, s55\n"
    "ds_write_b32 v120, v5 offset:144\n"
    "s_or_b32 s52, s52, s54\n"
    "s_add_u32 s52, s52, 1\n"
    "s_and_b32 s52, s52, s53\n"
    "s_waitcnt vmcnt(8)\n"
    ".Lt_86_%=:\n"
    "s_memrealtime s[58:59]\n"
    "s_waitcnt lgkmcnt(0)\n"
    "s_sub_u32 s58, s58, s57\n"
    "s_cmpk_lt_i32 s58, 514\n"
    "s_cbranch_scc1 .Lt_86_%=\n"
    "buffer_load_dwordx4 v[60:63], v1, s[20:23], s52 offen nt\n"
    "s_getreg_b32 s84, hwreg(7, 0, 4)\n"
    "s_cmp_gt_u32 s84, 5\n"
    "s_cselect_b32 s84, 5, -1\n"
    "s_add_i32 s55, s55, s84\n"
    "s_max_i32 s55, s55, 0\n"
    "s_lshr_b32 s84, s55, 2\n"
    "s_add_u32 s57, s56, s84\n"
    "v_mov_b32 v5, s55\n"
    "ds_write_b32 v120, v5 offset:144\n"
    "s_or_b32 s52, s52, s54\n"
    "s_add_u32 s52, s52, 1\n"
    "s_and_b32 s52, s52, s53\n"
    "s_waitcnt vmcnt(8)\n"
    ".Lt_87_%=:\n"
    "s_memrealtime s[58:59]\n"
    "s_waitcnt lgkmcnt(0)\n"
    "s_sub_u32 s58, s58, s57\n"
    "s_cmpk_lt_i32 s58, 528\n"
    "s_cbranch_scc1 .Lt_87_%=\n"
    "buffer_load_dwordx4 v[64:67], v1, s[20:23], s52 offen nt\n"
    "s_getreg_b32 s84, hwreg(7, 0, 4)\n"
    "s_cmp_gt_u32 s84, 5\n"
    "s_cselect_b32 s84, 5, -1\n"
    "s_add_i32 s55, s55, s84\n"
    "s_max_i32 s55, s55, 0\n"
    "s_lshr_b32 s84, s55, 2\n"
    "s_add_u32 s57, s56, s84\n"
    "v_mov_b32 v5, s55\n"
    "ds_write_b32 v120, v5 offset:144\n"
    "s_or_b32 s52, s52, s54\n"
    "s_add_u32 s52, s52, 1\n"
    "s_and_b32 s52, s52, s53\n"
    "s_waitcnt vmcnt(8)\n"
    ".Lt_88_%=:\n"
    "s_memrealtime s[58:59]\n"
    "s_waitcnt lgkmcnt(0)\n"
    "s_sub_u32 s58, s58, s57\n"
    "s_cmpk_lt_i32 s58, 543\n"
    "s_cbranch_scc1 .Lt_88_%=\n"
    "buffer_load_dwordx4 v[68:71], v1, s[20:23], s52 offen nt\n"
    "s_getreg_b32 s84, hwreg(7, 0, 4)\n"
    "s_cmp_gt_u32 s84, 5\n"
    "s_cselect_b32 s84, 5, -1\n"
    "s_add_i32 s55, s55, s84\n"
    "s_max_i32 s55, s55, 0\n"
    "s_lshr_b32 s84, s55, 2\n"
    "s_add_u32 s57, s56, s84\n"
    "v_mov_b32 v5, s55\n"
    "ds_write_b32 v120, v5 offset:144\n"
    "s_mov_b32 s56, s57\n"
    ".Ls_89_%=:\n"
    "ds_read_b32 v5, v120 offset:64\n"
    "ds_read_b128 v[104:107], v128 offset:1536\n"
    "ds_read_b128 v[108:111], v128 offset:1552\n"
    "s_waitcnt lgkmcnt(0)\n"
    "v_cmp_ne_u32 vcc, 1, v5\n"
    "s_cbranch_vccnz .Lr_90_%=\n"
    ".Lk_91_%=:\n"
    "v_perm_b32 v72, v104, v104, v124\n"
    "v_perm_b32 v73, v104, v104, v125\n"
    "v_perm_b32 v74, v104, v104, v126\n"
    "v_perm_b32 v75, v104, v104, v127\n"
    "v_mfma_f32_16x16x16_bf16 v[112:115], v[72:73], v[8:9], 0\n"
    "v_perm_b32 v76, v105, v105, v124\n"
    "v_perm_b32 v77, v105, v105, v125\n"
    "v_mfma_f32_16x16x16_bf16 v[116:119], v[72:73], v[10:11], 0\n"
    "v_perm_b32 v78, v105, v105, v126\n"
    "v_perm_b32 v79, v105, v105, v127\n"
    "v_mfma_f32_16x16x16_bf16 v[112:115], v[74:75], v[12:13], v[112:115]\n"
    "v_perm_b32 v80, v106, v106, v124\n"
    "v_perm_b32 v81, v106, v106, v125\n"
    "v_mfma_f32_16x16x16_bf16 v[116:119], v[74:75], v[14:15], v[116:119]\n"
    "v_perm_b32 v82, v106, v106, v126\n"
    "v_perm_b32 v83, v106, v106, v127\n"
    "v_mfma_f32_16x16x16_bf16 v[112:115], v[76:77], v[16:17], v[112:115]\n"
    "v_perm_b32 v84, v107, v107, v124\n"
    "v_perm_b32 v85, v107, v107, v125\n"
    "v_mfma_f32_16x16x16_bf16 v[116:119], v[76:77], v[18:19], v[116:119]\n"
    "v_perm_b32 v86, v107, v107, v126\n"
    "v_perm_b32 v87, v107, v107, v127\n"
    "v_mfma_f32_16x16x16_bf16 v[112:115], v[78:79], v[20:21], v[112:115]\n"
    "v_perm_b32 v88, v108, v108, v124\n"
    "v_perm_b32 v89, v108, v108, v125\n"
    "v_mfma_f32_16x16x16_bf16 v[116:119], v[78:79], v[22:23], v[116:119]\n"
    "v_perm_b32 v90, v108, v108, v126\n"
    "v_perm_b32 v91, v108, v108, v127\n"
    "v_mfma_f32_16x16x16_bf16 v[112:115], v[80:81], v[24:25], v[112:115]\n"
    "v_perm_b32 v92, v109, v109, v124\n"
    "v_perm_b32 v93, v109, v109, v125\n"
    "v_mfma_f32_16x16x16_bf16 v[116:119], v[80:81], v[26:27], v[116:119]\n"
    "v_perm_b32 v94, v109, v109, v126\n"
    "v_perm_b32 v95, v109, v109, v127\n"
    "v_mfma_f32_16x16x16_bf16 v[112:115], v[82:83], v[28:29], v[112:115]\n"
    "v_perm_b32 v96, v110, v110, v124\n"
    "v_perm_b32 v97, v110, v110, v125\n"
    "v_mfma_f32_16x16x16_bf16 v[116:119], v[82:83], v[30:31], v[116:119]\n"
    "v_perm_b32 v98, v110, v110, v126\n"
    "v_perm_b32 v99, v110, v110, v127\n"
    "v_mfma_f32_16x16x16_bf16 v[112:115], v[84:85], v[32:33], v[112:115]\n"
    "v_perm_b32 v100, v111, v111, v124\n"
    "v_perm_b32 v101, v111, v111, v125\n"
    "v_mfma_f32_16x16x16_bf16 v[116:119], v[84:85], v[34:35], v[116:119]\n"
    "v_perm_b32 v102, v111, v111, v126\n"
    "v_perm_b32 v103, v111, v111, v127\n"
    "s_waitcnt vmcnt(8)\n"
    "v_mfma_f32_16x16x16_bf16 v[112:115], v[86:87], v[36:37], v[112:115]\n"
    "v_mfma_f32_16x16x16_bf16 v[116:119], v[86:87], v[38:39], v[116:119]\n"
    "s_waitcnt vmcnt(7)\n"
    "v_mfma_f32_16x16x16_bf16 v[112:115], v[88:89], v[40:41], v[112:115]\n"
    "v_mfma_f32_16x16x16_bf16 v[116:119], v[88:89], v[42:43], v[116:119]\n"
    "s_waitcnt vmcnt(6)\n"
    "v_mfma_f32_16x16x16_bf16 v[112:115], v[90:91], v[44:45], v[112:115]\n"
    "v_mfma_f32_16x16x16_bf16 v[116:119], v[90:91], v[46:47], v[116:119]\n"
    "s_waitcnt vmcnt(5)\n"
    "v_mfma_f32_16x16x16_bf16 v[112:115], v[92:93], v[48:49], v[112:115]\n"
    "v_mfma_f32_16x16x16_bf16 v[116:119], v[92:93], v[50:51], v[116:119]\n"
    "s_waitcnt vmcnt(4)\n"
    "v_mfma_f32_16x16x16_bf16 v[112:115], v[94:95], v[52:53], v[112:115]\n"
    "v_mfma_f32_16x16x16_bf16 v[116:119], v[94:95], v[54:55], v[116:119]\n"
    "s_waitcnt vmcnt(3)\n"
    "v_mfma_f32_16x16x16_bf16 v[112:115], v[96:97], v[56:57], v[112:115]\n"
    "v_mfma_f32_16x16x16_bf16 v[116:119], v[96:97], v[58:59], v[116:119]\n"
    "s_waitcnt vmcnt(2)\n"
    "v_mfma_f32_16x16x16_bf16 v[112:115], v[98:99], v[60:61], v[112:115]\n"
    "v_mfma_f32_16x16x16_bf16 v[116:119], v[98:99], v[62:63], v[116:119]\n"
    "s_waitcnt vmcnt(1)\n"
    "v_mfma_f32_16x16x16_bf16 v[112:115], v[100:101], v[64:65], v[112:115]\n"
    "v_mfma_f32_16x16x16_bf16 v[116:119], v[100:101], v[66:67], v[116:119]\n"
    "s_waitcnt vmcnt(0)\n"
    "v_mfma_f32_16x16x16_bf16 v[112:115], v[102:103], v[68:69], v[112:115]\n"
    "v_mfma_f32_16x16x16_bf16 v[116:119], v[102:103], v[70:71], v[116:119]\n"
    "s_nop 6\n"
    "ds_write_b128 v129, v[112:115] offset:7680\n"
    "ds_write_b128 v129, v[116:119] offset:8768\n"
    "ds_write_b32 v120, v121 offset:48\n"
    ".Ls_92_%=:\n"
    "ds_read_b32 v5, v122 offset:48\n"
    "ds_read2st64_b32 v[8:9], v130 offset0:30 offset1:39\n"
    "ds_read2st64_b32 v[10:11], v131 offset0:30 offset1:39\n"
    "ds_read2st64_b32 v[12:13], v130 offset0:48 offset1:57\n"
    "ds_read2st64_b32 v[14:15], v131 offset0:48 offset1:57\n"
    "s_waitcnt lgkmcnt(0)\n"
    "v_cmp_ne_u32 vcc, 1, v5\n"
    "s_cbranch_vccnz .Lr_93_%=\n"
    ".Lk_94_%=:\n"
    "v_pk_add_f32 v[8:9], v[8:9], v[12:13]\n"
    "v_pk_add_f32 v[10:11], v[10:11], v[14:15]\n"
    "v_add_f32 v8, v8, v9\n"
    "v_add_f32 v9, v10, v11\n"
    "v_cvt_pk_bf16_f32 v2, v8, v9\n"
    ".Lt_95_%=:\n"
    "s_memrealtime s[58:59]\n"
    "s_waitcnt lgkmcnt(0)\n"
    "s_sub_u32 s58, s58, s56\n"
    "s_cmpk_lt_i32 s58, 645\n"
    "s_cbranch_scc1 .Lt_95_%=\n"
    "buffer_store_dwordx2 v[2:3], v132, s[16:19], s95 offen sc1\n"
    "s_lshl2_add_u32 s84, s2, s42\n"
    "s_cmp_lg_u32 s84, 1\n"
    "s_cbranch_scc1 .Le_96_%=\n"
    ".Lt_98_%=:\n"
    "s_memrealtime s[58:59]\n"
    "s_waitcnt lgkmcnt(0)\n"
    "s_sub_u32 s58, s58, s56\n"
    "s_cmpk_lt_i32 s58, 675\n"
    "s_cbranch_scc1 .Lt_98_%=\n"
    "v_lshlrev_b32 v4, 3, v123\n"
    "s_add_u32 s84, s48, 1024\n"
    "s_add_u32 s85, s84, 8192\n"
    ".Le_97_%=:\n"
    "buffer_load_dword v5, v4, s[16:19], s84 offen offset:4 sc1\n"
    "buffer_load_dword v6, v4, s[16:19], s85 offen offset:4 sc1\n"
    "s_waitcnt vmcnt(0)\n"
    "v_xor_b32 v5, s40, v5\n"
    "v_xor_b32 v6, s40, v6\n"
    "v_or_b32 v5, v5, v6\n"
    "v_cmp_ne_u32 vcc, 0, v5\n"
    "s_cbranch_vccnz .Lr_99_%=\n"
    ".Lk_100_%=:\n"
    "s_mov_b64 exec, 1\n"
    "buffer_store_dword v3, off, s[16:19], 0 offset:0 sc1\n"
    "s_mov_b64 exec, -1\n"
    ".Le_96_%=:\n"
    "s_barrier\n"
    "ds_read2st64_b32 v[8:9], v133 offset0:0 offset1:2\n"
    "ds_read2st64_b32 v[10:11], v133 offset0:8 offset1:10\n"
    "ds_read2st64_b32 v[12:13], v133 offset0:16 offset1:18\n"
    "ds_read2st64_b32 v[14:15], v133 offset0:24 offset1:26\n"
    "s_waitcnt lgkmcnt(2)\n"
    "v_pk_add_f32 v[8:9], v[8:9], v[10:11]\n"
    "s_waitcnt lgkmcnt(0)\n"
    "v_pk_add_f32 v[12:13], v[12:13], v[14:15]\n"
    "v_pk_add_f32 v[8:9], v[8:9], v[12:13]\n"
    "v_add_f32 v2, v8, v9\n"
    "buffer_store_dwordx2 v[2:3], v134, s[16:19], s96 offen sc1\n"
    "s_endpgm\n"
    ".Lr_64_%=:\n"
    "s_sub_i32 s41, s41, 1\n"
    "s_cmp_gt_i32 s41, 0\n"
    "s_cbranch_scc0 .Lk_65_%=\n"
    "s_branch .Ls_63_%=\n"
    ".Lr_70_%=:\n"
    "s_sub_i32 s41, s41, 1\n"
    "s_cmp_gt_i32 s41, 0\n"
    "s_cbranch_scc0 .Lk_71_%=\n"
    "s_sleep 1\n"
    "s_branch .Ls_69_%=\n"
    ".Lr_90_%=:\n"
    "s_sub_i32 s41, s41, 1\n"
    "s_cmp_gt_i32 s41, 0\n"
    "s_cbranch_scc0 .Lk_91_%=\n"
    "s_branch .Ls_89_%=\n"
    ".Lr_93_%=:\n"
    "s_sub_i32 s41, s41, 1\n"
    "s_cmp_gt_i32 s41, 0\n"
    "s_cbranch_scc0 .Lk_94_%=\n"
    "s_branch .Ls_92_%=\n"
    ".Lr_99_%=:\n"
    "s_sub_i32 s41, s41, 1\n"
    "s_cmp_gt_i32 s41, 0\n"
    "s_cbranch_scc0 .Lk_100_%=\n"
    "s_sleep 4\n"
    "s_branch .Le_97_%=\n"
    ".Lfin_%=:\n"
    : "+{v0}"(tid)
    : "{s[0:1]}"(__builtin_amdgcn_kernarg_segment_ptr()), "{s2}"(blockIdx.x)
    : "v1", "v2", "v3", "v4", "v5", "v6", "v7", "v8", "v9", "v10", "v11", "v12", "v13", "v14", "v15", "v16", "v17", "v18",
      "v19", "v20", "v21", "v22", "v23", "v24", "v25", "v26", "v27", "v28", "v29", "v30", "v31", "v32", "v33", "v34", "v35", "v36",
      "v37", "v38", "v39", "v40", "v41", "v42", "v43", "v44", "v45", "v46", "v47", "v48", "v49", "v50", "v51", "v52", "v53", "v54",
      "v55", "v56", "v57", "v58", "v59", "v60", "v61", "v62", "v63", "v64", "v65", "v66", "v67", "v68", "v69", "v70", "v71", "v72",
      "v73", "v74", "v75", "v76", "v77", "v78", "v79", "v80", "v81", "v82", "v83", "v84", "v85", "v86", "v87", "v88", "v89", "v90",
      "v91", "v92", "v93", "v94", "v95", "v96", "v97", "v98", "v99", "v100", "v101", "v102", "v103", "v104", "v105", "v106", "v107", "v108",
      "v109", "v110", "v111", "v112", "v113", "v114", "v115", "v116", "v117", "v118", "v119", "v120", "v121", "v122", "v123", "v124", "v125", "v126",
      "v127", "v128", "v129", "v130", "v131", "v132", "v133", "v134", "v135", "s3", "s4", "s5", "s6", "s7", "s8", "s9", "s10", "s11",
      "s12", "s13", "s14", "s15", "s16", "s17", "s18", "s19", "s20", "s21", "s22", "s23", "s24", "s25", "s26", "s27", "s28", "s29",
      "s30", "s31", "s33", "s34", "s35", "s36", "s37", "s38", "s39", "s40", "s41", "s42", "s43", "s44", "s45", "s46", "s47", "s48",
      "s49", "s50", "s51", "s52", "s53", "s54", "s55", "s56", "s57", "s58", "s59", "s60", "s61", "s62", "s63", "s64", "s65", "s66",
      "s67", "s68", "s69", "s70", "s71", "s72", "s73", "s74", "s75", "s76", "s77", "s78", "s79", "s80", "s81", "s82", "s83", "s84",
      "s85", "s86", "s87", "s88", "s89", "s90", "s91", "s92", "s93", "s94", "s95", "s96", "s97", "s98", "vcc", "scc", "memory");
  asm volatile("" :: "v"((__attribute__((address_space(3))) char*)smem));
}

extern "C" void kernel_launch(void* const* d_in, const int* in_sizes, int n_in, void* d_out, int out_size, void* d_ws,
                              size_t ws_size, hipStream_t stream) {
  constexpr unsigned WIN0 = 6144u * 256u;
  Params P;
  for (int i = 0; i < 2; ++i) {
    const int o = 1 - i;
    P.s[i] = Side{(const char*)d_in[5 * i + 2] + WIN0, d_in[5 * o], d_in[5 * o + 4], d_in[5 * o + 3], (const char*)d_out + i * 1024,
                  (const char*)d_in[5 * i + 1] + WIN0, d_ws, d_ws};
  }
  fused_decode<<<256, 512, 0, stream>>>(P);
}
```
